```python
import torch

def _softmax(x, axis=-1):
    return torch.softmax(x, dim=axis)
def _gelu(x):
    return torch.nn.functional.gelu(x, approximate="tanh")
def _silu(x):
    return torch.nn.functional.silu(x)
def _sigmoid(x):
    return torch.sigmoid(x)
def _layernorm(x, axis=-1, eps=1e-5):
    m = x.mean(dim=axis, keepdim=True)
    v = x.var(dim=axis, keepdim=True, unbiased=False)
    return (x - m) / torch.sqrt(v + eps)
def _scatter_add(tgt, idx, src, axis=0):
    return tgt.clone().index_add_(axis, idx, src)
def _segment_sum(src, seg, n):
    out = torch.zeros((n,) + src.shape[1:], dtype=src.dtype)
    return out.index_add_(0, seg, src)
def _pad(x, pw):
    flat = tuple(v for pair in reversed(pw) for v in pair)
    return torch.nn.functional.pad(x, flat)

kernel_name = 'sequential_preLN__sliding_block_cached__geglu_ffn__decode'

def setup_inputs():

    _w = (lambda a: (lambda t: t.to(torch.bfloat16) if t.is_floating_point() else t)(torch.as_tensor(a)))
    def _A_setup():
        B, Sq, D = 2, 1, 640
        Hq, Hkv, Sk, Dh = 32, 32, 8192, 192
        assert Hq % Hkv == 0, (Hq, Hkv)
        return {
            "x": _w((lambda *s: torch.randn(*s, dtype=torch.bfloat16) * 0.05)(B, Sq, D)),
            "k_cache": _w((lambda *s: torch.randn(*s, dtype=torch.bfloat16) * 3.2)(B, Hkv, Sk, Dh)),
            "v_cache": _w((lambda *s: torch.randn(*s, dtype=torch.bfloat16) * 0.4)(B, Hkv, Sk, Dh)),
            "wq": _w((lambda *s: torch.randn(*s, dtype=torch.bfloat16) * 3.2)(D, Hq * Dh) * (D ** -0.5)),
            "wo": _w((lambda *s: torch.randn(*s, dtype=torch.bfloat16) * 0.4)(Hq * Dh, D) * ((Hq * Dh) ** -0.5)),
        }
    _A_in = _A_setup()

    def _B_setup():
        B, S, D, F = 2, 1, 640, 1536
        return {
            "x": _w((lambda *s: torch.randn(*s, dtype=torch.bfloat16) * 0.05)(B, S, D)),
            "w_gate": _w((lambda *s: torch.randn(*s, dtype=torch.bfloat16) * 0.05)(D, F)),
            "w_up": _w((lambda *s: torch.randn(*s, dtype=torch.bfloat16) * 0.05)(D, F)),
            "w_down": _w((lambda *s: torch.randn(*s, dtype=torch.bfloat16) * 0.05)(F, D)),
        }
    _B_in = _B_setup()

    return {
        "A_x": _A_in["x"],
        "A_k_cache": _A_in["k_cache"],
        "A_v_cache": _A_in["v_cache"],
        "A_wq": _A_in["wq"],
        "A_wo": _A_in["wo"],
        "B_x": _B_in["x"],
        "B_w_gate": _B_in["w_gate"],
        "B_w_up": _B_in["w_up"],
        "B_w_down": _B_in["w_down"],
    }

def reference(A_x, A_k_cache, A_v_cache, A_wq, A_wo, B_x, B_w_gate, B_w_up, B_w_down):
    def _A_ref(x, k_cache, v_cache, wq, wo):
        B, Sq, D = x.shape
        Hq, Dh = 32, 192
        Hkv, Sk = k_cache.shape[1], k_cache.shape[2]
        W = min(4096, max(256, Sk // 4))
        rep = Hq // Hkv
        q = (lambda x, *d: x.permute(*d))((x @ wq).reshape(B, Sq, Hq, Dh), 0, 2, 1, 3)
        k = (lambda x, n, axis: torch.repeat_interleave(x, n, dim=axis))(k_cache[:, :, Sk - W:, :], rep, axis=1)
        v = (lambda x, n, axis: torch.repeat_interleave(x, n, dim=axis))(v_cache[:, :, Sk - W:, :], rep, axis=1)
        scores = torch.einsum("bhqd,bhkd->bhqk", q, k) * (Dh ** -0.5)
        p = _softmax(scores, axis=-1)
        o = torch.einsum("bhqk,bhkd->bhqd", p, v)
        return (lambda x, *d: x.permute(*d))(o, 0, 2, 1, 3).reshape(B, Sq, Hq * Dh) @ wo

    def _B_ref(x, w_gate, w_up, w_down):
        return (_gelu(x @ w_gate) * (x @ w_up)) @ w_down

    x = A_x
    h = x + _A_ref(_layernorm(x, axis=-1), A_k_cache, A_v_cache, A_wq, A_wo)
    h2 = h + _B_ref(_layernorm(h, axis=-1), B_w_gate, B_w_up, B_w_down)
    return h2
```

```cpp
#include <hip/hip_runtime.h>
#include <hip/hip_bf16.h>
#include <stdint.h>

namespace {
constexpr int B = 2, D = 640, H = 32, SK = 8192, DH = 192, W = 2048, F = 1536, QN = H * DH;
constexpr int NSPLIT = 4, KPS = W / NSPLIT;

__device__ __forceinline__ float bf2f(uint16_t v) { return __uint_as_float(((unsigned)v) << 16); }
__device__ __forceinline__ float rbf(float f) { return __bfloat162float(__float2bfloat16(f)); }

__device__ void layernorm640(const float* xin, float* xn, float* red, int tid) {
  float s = 0.f;
  for (int i = tid; i < D; i += 256) s += xin[i];
  for (int o = 32; o > 0; o >>= 1) s += __shfl_xor(s, o);
  if ((tid & 63) == 0) red[tid >> 6] = s;
  __syncthreads();
  float mean = (red[0] + red[1] + red[2] + red[3]) / D;
  __syncthreads();
  float q = 0.f;
  for (int i = tid; i < D; i += 256) { float d = xin[i] - mean; q += d * d; }
  for (int o = 32; o > 0; o >>= 1) q += __shfl_xor(q, o);
  if ((tid & 63) == 0) red[tid >> 6] = q;
  __syncthreads();
  float var = (red[0] + red[1] + red[2] + red[3]) / D;
  float mb = rbf(mean), vb = rbf(var);
  float den = rbf(sqrtf(rbf(vb + 1e-5f)));
  for (int i = tid; i < D; i += 256) xn[i] = rbf(rbf(xin[i] - mb) / den);
  __syncthreads();
}

__global__ void __launch_bounds__(256) k_q(const uint16_t* x, const uint16_t* wq, float* q) {
  __shared__ float xin[D], xn[D], red[4], part[32][64];
  int b = blockIdx.y, c0 = blockIdx.x * 64, tid = threadIdx.x;
  for (int i = tid; i < D; i += 256) xin[i] = bf2f(x[b * D + i]);
  __syncthreads();
  layernorm640(xin, xn, red, tid);
  int cc = tid & 7, rg = tid >> 3;
  float acc[8] = {0};
  for (int r = rg * 20; r < rg * 20 + 20; ++r) {
    uint4 w = *(const uint4*)(wq + (size_t)r * QN + c0 + cc * 8);
    unsigned ww[4] = {w.x, w.y, w.z, w.w};
    for (int k = 0; k < 4; ++k) {
      acc[2 * k] += xn[r] * __uint_as_float(ww[k] << 16);
      acc[2 * k + 1] += xn[r] * __uint_as_float(ww[k] & 0xffff0000u);
    }
  }
  for (int k = 0; k < 8; ++k) part[rg][cc * 8 + k] = acc[k];
  __syncthreads();
  if (tid < 64) {
    float s = 0.f;
    for (int g = 0; g < 32; ++g) s += part[g][tid];
    q[b * QN + c0 + tid] = rbf(s);
  }
}

__global__ void __launch_bounds__(256) k_attn(const float* q, const uint16_t* kc, const uint16_t* vc, float* po, float* pml) {
  __shared__ float qs[DH], sc[KPS], red[8], ov[8][DH];
  int s = blockIdx.x, h = blockIdx.y, b = blockIdx.z, tid = threadIdx.x;
  if (tid < DH) qs[tid] = q[b * QN + h * DH + tid];
  __syncthreads();
  const uint16_t* kb = kc + ((size_t)(b * H + h) * SK + (SK - W) + s * KPS) * DH;
  const uint16_t* vb = vc + ((size_t)(b * H + h) * SK + (SK - W) + s * KPS) * DH;
  for (int j = tid; j < KPS; j += 256) {
    float d = 0.f;
    for (int i = 0; i < DH; i += 8) {
      uint4 w = *(const uint4*)(kb + (size_t)j * DH + i);
      unsigned ww[4] = {w.x, w.y, w.z, w.w};
      for (int k = 0; k < 4; ++k) {
        d += qs[i + 2 * k] * __uint_as_float(ww[k] << 16);
        d += qs[i + 2 * k + 1] * __uint_as_float(ww[k] & 0xffff0000u);
      }
    }
    sc[j] = rbf(rbf(d) * 0.07216878364870322f);
  }
  __syncthreads();
  float m = -INFINITY;
  for (int j = tid; j < KPS; j += 256) m = fmaxf(m, sc[j]);
  for (int o = 32; o > 0; o >>= 1) m = fmaxf(m, __shfl_xor(m, o));
  if ((tid & 63) == 0) red[tid >> 6] = m;
  __syncthreads();
  m = fmaxf(fmaxf(red[0], red[1]), fmaxf(red[2], red[3]));
  __syncthreads();
  float l = 0.f;
  for (int j = tid; j < KPS; j += 256) { float p = __expf(sc[j] - m); sc[j] = p; l += p; }
  for (int o = 32; o > 0; o >>= 1) l += __shfl_xor(l, o);
  if ((tid & 63) == 0) red[4 + (tid >> 6)] = l;
  __syncthreads();
  l = red[4] + red[5] + red[6] + red[7];
  int cc = tid % 24, rg = tid / 24;
  if (rg < 8) {
    float acc[8] = {0};
    for (int j = rg; j < KPS; j += 8) {
      uint4 w = *(const uint4*)(vb + (size_t)j * DH + cc * 8);
      unsigned ww[4] = {w.x, w.y, w.z, w.w};
      float p = sc[j];
      for (int k = 0; k < 4; ++k) {
        acc[2 * k] += p * __uint_as_float(ww[k] << 16);
        acc[2 * k + 1] += p * __uint_as_float(ww[k] & 0xffff0000u);
      }
    }
    for (int k = 0; k < 8; ++k) ov[rg][cc * 8 + k] = acc[k];
  }
  __syncthreads();
  size_t slot = ((size_t)(b * H + h) * NSPLIT + s);
  if (tid < DH) {
    float t = 0.f;
    for (int g = 0; g < 8; ++g) t += ov[g][tid];
    po[slot * DH + tid] = t;
  }
  if (tid == 0) { pml[slot * 2] = m; pml[slot * 2 + 1] = l; }
}

__global__ void __launch_bounds__(256) k_wo(const float* po, const float* pml, const uint16_t* wo, float* y) {
  __shared__ float o[DH], part[3][D];
  int h = blockIdx.x, b = blockIdx.y, tid = threadIdx.x;
  size_t slot = (size_t)(b * H + h) * NSPLIT;
  if (tid < DH) {
    float M = -INFINITY;
    for (int s = 0; s < NSPLIT; ++s) M = fmaxf(M, pml[(slot + s) * 2]);
    float L = 0.f, a = 0.f;
    for (int s = 0; s < NSPLIT; ++s) {
      float w = __expf(pml[(slot + s) * 2] - M);
      L += w * pml[(slot + s) * 2 + 1];
      a += w * po[(slot + s) * DH + tid];
    }
    o[tid] = rbf(a / L);
  }
  __syncthreads();
  int cc = tid % 80, rg = tid / 80;
  if (rg < 3) {
    float acc[8] = {0};
    for (int r = rg * 64; r < rg * 64 + 64; ++r) {
      uint4 w = *(const uint4*)(wo + (size_t)(h * DH + r) * D + cc * 8);
      unsigned ww[4] = {w.x, w.y, w.z, w.w};
      for (int k = 0; k < 4; ++k) {
        acc[2 * k] += o[r] * __uint_as_float(ww[k] << 16);
        acc[2 * k + 1] += o[r] * __uint_as_float(ww[k] & 0xffff0000u);
      }
    }
    for (int k = 0; k < 8; ++k) part[rg][cc * 8 + k] = acc[k];
  }
  __syncthreads();
  for (int c = tid; c < D; c += 256) y[((size_t)b * H + h) * D + c] = part[0][c] + part[1][c] + part[2][c];
}

__global__ void __launch_bounds__(256) k_up(const uint16_t* x, const float* y, const uint16_t* wg, const uint16_t* wu, float* hout, float* g) {
  __shared__ float hin[D], hn[D], red[4], pg[32][64], pu[32][64];
  int b = blockIdx.y, c0 = blockIdx.x * 64, tid = threadIdx.x;
  for (int i = tid; i < D; i += 256) {
    float s = 0.f;
    for (int h = 0; h < H; ++h) s += y[((size_t)b * H + h) * D + i];
    float hv = rbf(bf2f(x[b * D + i]) + rbf(s));
    hin[i] = hv;
    if (blockIdx.x == 0) hout[b * D + i] = hv;
  }
  __syncthreads();
  layernorm640(hin, hn, red, tid);
  int cc = tid & 7, rg = tid >> 3;
  float ag[8] = {0}, au[8] = {0};
  for (int r = rg * 20; r < rg * 20 + 20; ++r) {
    uint4 w = *(const uint4*)(wg + (size_t)r * F + c0 + cc * 8);
    uint4 u = *(const uint4*)(wu + (size_t)r * F + c0 + cc * 8);
    unsigned ww[4] = {w.x, w.y, w.z, w.w}, uu[4] = {u.x, u.y, u.z, u.w};
    for (int k = 0; k < 4; ++k) {
      ag[2 * k] += hn[r] * __uint_as_float(ww[k] << 16);
      ag[2 * k + 1] += hn[r] * __uint_as_float(ww[k] & 0xffff0000u);
      au[2 * k] += hn[r] * __uint_as_float(uu[k] << 16);
      au[2 * k + 1] += hn[r] * __uint_as_float(uu[k] & 0xffff0000u);
    }
  }
  for (int k = 0; k < 8; ++k) { pg[rg][cc * 8 + k] = ag[k]; pu[rg][cc * 8 + k] = au[k]; }
  __syncthreads();
  if (tid < 64) {
    float sg = 0.f, su = 0.f;
    for (int q = 0; q < 32; ++q) { sg += pg[q][tid]; su += pu[q][tid]; }
    sg = rbf(sg); su = rbf(su);
    float t = 0.7978845608028654f * (sg + 0.044715f * sg * sg * sg);
    float ge = rbf(0.5f * sg * (1.f + tanhf(t)));
    g[b * F + c0 + tid] = rbf(ge * su);
  }
}

__global__ void __launch_bounds__(256) k_down(const float* hout, const float* g, const uint16_t* wd, __hip_bfloat16* out) {
  __shared__ float gs[F], part[32][64];
  int b = blockIdx.y, c0 = blockIdx.x * 64, tid = threadIdx.x;
  for (int i = tid; i < F; i += 256) gs[i] = g[b * F + i];
  __syncthreads();
  int cc = tid & 7, rg = tid >> 3;
  float acc[8] = {0};
  for (int r = rg * 48; r < rg * 48 + 48; ++r) {
    uint4 w = *(const uint4*)(wd + (size_t)r * D + c0 + cc * 8);
    unsigned ww[4] = {w.x, w.y, w.z, w.w};
    for (int k = 0; k < 4; ++k) {
      acc[2 * k] += gs[r] * __uint_as_float(ww[k] << 16);
      acc[2 * k + 1] += gs[r] * __uint_as_float(ww[k] & 0xffff0000u);
    }
  }
  for (int k = 0; k < 8; ++k) part[rg][cc * 8 + k] = acc[k];
  __syncthreads();
  if (tid < 64) {
    float s = 0.f;
    for (int q = 0; q < 32; ++q) s += part[q][tid];
    out[b * D + c0 + tid] = __float2bfloat16(hout[b * D + c0 + tid] + rbf(s));
  }
}
}

extern "C" void kernel_launch(void* const* d_in, const int* in_sizes, int n_in, void* d_out, int out_size,
                              void* d_ws, size_t ws_size, hipStream_t stream) {
  const uint16_t* x = (const uint16_t*)d_in[0];
  const uint16_t* kc = (const uint16_t*)d_in[1];
  const uint16_t* vc = (const uint16_t*)d_in[2];
  const uint16_t* wq = (const uint16_t*)d_in[3];
  const uint16_t* wo = (const uint16_t*)d_in[4];
  const uint16_t* wg = (const uint16_t*)d_in[6];
  const uint16_t* wu = (const uint16_t*)d_in[7];
  const uint16_t* wd = (const uint16_t*)d_in[8];
  float* ws = (float*)d_ws;
  float* q = ws;
  float* po = q + B * QN;
  float* pml = po + B * H * NSPLIT * DH;
  float* y = pml + B * H * NSPLIT * 2;
  float* hout = y + B * H * D;
  float* g = hout + B * D;
  k_q<<<dim3(QN / 64, B), 256, 0, stream>>>(x, wq, q);
  k_attn<<<dim3(NSPLIT, H, B), 256, 0, stream>>>(q, kc, vc, po, pml);
  k_wo<<<dim3(H, B), 256, 0, stream>>>(po, pml, wo, y);
  k_up<<<dim3(F / 64, B), 256, 0, stream>>>(x, y, wg, wu, hout, g);
  k_down<<<dim3(D / 64, B), 256, 0, stream>>>(hout, g, wd, (__hip_bfloat16*)d_out);
}
```

```cpp
#include <hip/hip_runtime.h>
#include <hip/hip_bf16.h>
#include <stdint.h>

namespace {
typedef unsigned u32;
typedef u32 u32x2 __attribute__((ext_vector_type(2)));
typedef u32 u32x3 __attribute__((ext_vector_type(3)));
typedef u32 u32x4 __attribute__((ext_vector_type(4)));
typedef float f32x2 __attribute__((ext_vector_type(2)));
typedef float f32x4 __attribute__((ext_vector_type(4)));
typedef __bf16 bf16x2 __attribute__((ext_vector_type(2)));

constexpr int D = 640, H = 32, SK = 8192, DH = 192, W = 2048, F = 1536, QN = H * DH;
constexpr int NWG = 256, NT = 512, NRED = 160;
constexpr int SPIN_LIMIT = 1 << 20;

constexpr size_t WS_EPOCH = 0;
constexpr size_t WS_ML = 4096;
constexpr size_t WS_H = WS_ML + 256 * 16;
constexpr size_t WS_Y = WS_H + 640 * 16 + 2048;
constexpr size_t WS_Z = WS_Y + (size_t)NRED * 256 * 2 * 16;
constexpr size_t WS_END = WS_Z + (size_t)NRED * 256 * 4 * 16;

__device__ __forceinline__ float rbf(float f) { return __bfloat162float(__float2bfloat16(f)); }
__device__ __forceinline__ float lo2f(u32 v) { return __uint_as_float(v << 16); }
__device__ __forceinline__ float hi2f(u32 v) { return __uint_as_float(v & 0xffff0000u); }
__device__ __forceinline__ u32 pk2(float lo, float hi) {
  u32 r; asm("v_cvt_pk_bf16_f32 %0, %1, %2" : "=v"(r) : "v"(lo), "v"(hi)); return r;
}
__device__ __forceinline__ float dot2(u32 a, u32 b, float c) {
  return __builtin_amdgcn_fdot2_f32_bf16(__builtin_bit_cast(bf16x2, a), __builtin_bit_cast(bf16x2, b), c, false);
}
template <int CTRL> __device__ __forceinline__ float dpp(float x) {
  return __builtin_bit_cast(float, __builtin_amdgcn_mov_dpp(__builtin_bit_cast(int, x), CTRL, 0xf, 0xf, true));
}
constexpr int XOR1 = 0xB1, XOR2 = 0x4E, XOR7 = 0x141, XOR8 = 0x128;
__device__ __forceinline__ float sw16sum(float a, float b) {
  auto s = __builtin_amdgcn_permlane16_swap(__float_as_uint(a), __float_as_uint(b), false, false);
  return __uint_as_float(s[0]) + __uint_as_float(s[1]);
}
__device__ __forceinline__ float sw32sum(float a, float b) {
  auto s = __builtin_amdgcn_permlane32_swap(__float_as_uint(a), __float_as_uint(b), false, false);
  return __uint_as_float(s[0]) + __uint_as_float(s[1]);
}
__device__ __forceinline__ float wave_sum(float x) {
  x += dpp<XOR1>(x); x += dpp<XOR2>(x); x += dpp<XOR7>(x); x += dpp<XOR8>(x);
  x = sw16sum(x, x); x = sw32sum(x, x); return x;
}
__device__ __forceinline__ float wave_max(float x) {
  x = fmaxf(x, dpp<XOR1>(x)); x = fmaxf(x, dpp<XOR2>(x)); x = fmaxf(x, dpp<XOR7>(x)); x = fmaxf(x, dpp<XOR8>(x));
  auto s = __builtin_amdgcn_permlane16_swap(__float_as_uint(x), __float_as_uint(x), false, false);
  x = fmaxf(__uint_as_float(s[0]), __uint_as_float(s[1]));
  auto t = __builtin_amdgcn_permlane32_swap(__float_as_uint(x), __float_as_uint(x), false, false);
  return fmaxf(__uint_as_float(t[0]), __uint_as_float(t[1]));
}
__device__ __forceinline__ void st_gran(unsigned char* p, float v0, float v1, u32 ta, u32 tb) {
  u32x4 g = {__float_as_uint(v0), ta, __float_as_uint(v1), tb};
  asm volatile("global_store_dwordx4 %0, %1, off sc1\n\ts_nop 1" :: "v"(p), "v"(g) : "memory");
}
__device__ __forceinline__ u32x4 ld_gran(const unsigned char* p) {
  u32x4 g;
  asm volatile("global_load_dwordx4 %0, %1, off sc1\n\ts_waitcnt vmcnt(0)" : "=&v"(g) : "v"(p) : "memory");
  return g;
}
__device__ __forceinline__ void ld_gran2(const unsigned char* p, const unsigned char* q, u32x4& a, u32x4& b) {
  asm volatile("global_load_dwordx4 %0, %2, off sc1\n\tglobal_load_dwordx4 %1, %3, off sc1\n\ts_waitcnt vmcnt(0)"
               : "=&v"(a), "=&v"(b) : "v"(p), "v"(q) : "memory");
}
__device__ __forceinline__ __amdgpu_buffer_rsrc_t mkrsrc(const void* p, u32 bytes) {
  return __builtin_amdgcn_make_buffer_rsrc(const_cast<void*>(p), 0, bytes, 0x00020000);
}

struct Params {
  const uint16_t* x; const uint16_t* kc; const uint16_t* vc; const uint16_t* wq; const uint16_t* wo;
  const uint16_t* wg; const uint16_t* wu; const uint16_t* wd; uint16_t* out; unsigned char* ws;
};

struct Lds {
  u32 xnp[320];
  float qpart[20][192];
  u32 qpk[96];
  float pl[8][64];
  float ml[8][2];
  float red[16][192];
  u32 opk[96];
  float ypart[6][640];
  float wsum[8][4];
  float hs[2][640];
  float gpart[5][24];
  float gv[2][8];
  float zpart[6][2][640];
  float fin[8][8];
};

__global__ void __launch_bounds__(NT, 2) fused_kernel(Params P) {
  __shared__ __attribute__((aligned(16))) Lds S;
  const int tid = threadIdx.x, lane = tid & 63, wave = __builtin_amdgcn_readfirstlane(tid >> 6);
  const int half = lane >> 5, c = lane & 31;
  const int r4 = lane >> 4, c16 = lane & 15;
  const int wg = blockIdx.x, xcd = wg & 7, idx = wg >> 3;
  const int h = (idx >> 3) * 8 + xcd, b = (idx >> 2) & 1, sp = idx & 3;
  const int prod = b * 128 + h * 4 + sp;

  const unsigned long long ep = __hip_atomic_load((const unsigned long long*)(P.ws + WS_EPOCH), __ATOMIC_RELAXED, __HIP_MEMORY_SCOPE_AGENT);

  u32 xr[5];
  {
    const u32* xp = (const u32*)(P.x + b * D) + lane * 5;
#pragma unroll
    for (int i = 0; i < 5; ++i) xr[i] = xp[i];
  }
  const int qcc = tid % 24, qrg = tid / 24;
  u32x4 wqr[32];
  if (tid < 480) {
    const uint16_t* wp = P.wq + (size_t)(qrg * 32) * QN + h * DH + qcc * 8;
#pragma unroll
    for (int r = 0; r < 32; ++r) wqr[r] = *(const u32x4*)(wp + (size_t)r * QN);
  }
  const size_t kvoff = ((size_t)(b * H + h) * SK + (SK - W) + sp * 512 + wave * 64) * DH;
  const __amdgpu_buffer_rsrc_t krs = mkrsrc(P.kc + kvoff, 64 * 384);
  const __amdgpu_buffer_rsrc_t vrs = mkrsrc(P.vc + kvoff, 64 * 384);
  const u32 loA = r4 * 384 + c16 * 16, loB = r4 * 384 + 256 + c16 * 8;
  u32x4 ka[16]; u32x2 kb[16];
#pragma unroll
  for (int i = 0; i < 8; ++i) {
    ka[i] = __builtin_amdgcn_raw_buffer_load_b128(krs, loA, i * 1536, 2);
    kb[i] = __builtin_amdgcn_raw_buffer_load_b64(krs, loB, i * 1536, 2);
  }
  __builtin_amdgcn_sched_barrier(0);

  {
    float xv[10];
#pragma unroll
    for (int i = 0; i < 5; ++i) { xv[2 * i] = lo2f(xr[i]); xv[2 * i + 1] = hi2f(xr[i]); }
    float s = 0.f;
#pragma unroll
    for (int i = 0; i < 10; ++i) s += xv[i];
    const float mean = wave_sum(s) * (1.f / D);
    float q = 0.f;
#pragma unroll
    for (int i = 0; i < 10; ++i) { const float d = xv[i] - mean; q += d * d; }
    const float var = wave_sum(q) * (1.f / D);
    const float mb = rbf(mean), den = rbf(sqrtf(rbf(rbf(var) + 1e-5f)));
#pragma unroll
    for (int i = 0; i < 5; ++i) S.xnp[lane * 5 + i] = pk2(rbf(xv[2 * i] - mb) / den, rbf(xv[2 * i + 1] - mb) / den);
  }
  asm volatile("s_waitcnt lgkmcnt(0)" ::: "memory");
  if (tid < 480) {
    float acc[8];
#pragma unroll
    for (int k = 0; k < 8; ++k) acc[k] = 0.f;
#pragma unroll
    for (int j4 = 0; j4 < 4; ++j4) {
      const u32x4 xp = *(const u32x4*)&S.xnp[qrg * 16 + j4 * 4];
#pragma unroll
      for (int jj = 0; jj < 4; ++jj) {
        const u32x4 w0 = wqr[(j4 * 4 + jj) * 2], w1 = wqr[(j4 * 4 + jj) * 2 + 1];
#pragma unroll
        for (int k = 0; k < 4; ++k) {
          acc[2 * k] = dot2(__builtin_amdgcn_perm(w1[k], w0[k], 0x05040100), xp[jj], acc[2 * k]);
          acc[2 * k + 1] = dot2(__builtin_amdgcn_perm(w1[k], w0[k], 0x07060302), xp[jj], acc[2 * k + 1]);
        }
      }
    }
    *(f32x4*)&S.qpart[qrg][qcc * 8] = f32x4{acc[0], acc[1], acc[2], acc[3]};
    *(f32x4*)&S.qpart[qrg][qcc * 8 + 4] = f32x4{acc[4], acc[5], acc[6], acc[7]};
  }
  __builtin_amdgcn_sched_barrier(0);
#pragma unroll
  for (int i = 8; i < 16; ++i) {
    ka[i] = __builtin_amdgcn_raw_buffer_load_b128(krs, loA, i * 1536, 2);
    kb[i] = __builtin_amdgcn_raw_buffer_load_b64(krs, loB, i * 1536, 2);
  }
  u32x4 va[16]; u32x2 vb[16];
#pragma unroll
  for (int i = 0; i < 16; ++i) {
    va[i] = __builtin_amdgcn_raw_buffer_load_b128(vrs, loA, i * 1536, 2);
    vb[i] = __builtin_amdgcn_raw_buffer_load_b64(vrs, loB, i * 1536, 2);
  }
  __syncthreads();
  if (tid < 96) {
    float s0 = 0.f, s1 = 0.f;
#pragma unroll
    for (int g = 0; g < 20; ++g) { const f32x2 t = *(const f32x2*)&S.qpart[g][2 * tid]; s0 += t[0]; s1 += t[1]; }
    S.qpk[tid] = pk2(s0, s1);
  }
  __syncthreads();
  const u32x4 qa = *(const u32x4*)&S.qpk[4 * c16];
  const u32x2 qb = *(const u32x2*)&S.qpk[64 + 2 * c16];

  float d[16];
#pragma unroll
  for (int i = 0; i < 16; ++i) {
    float t = dot2(ka[i][0], qa[0], 0.f);
    t = dot2(ka[i][1], qa[1], t); t = dot2(ka[i][2], qa[2], t); t = dot2(ka[i][3], qa[3], t);
    t = dot2(kb[i][0], qb[0], t); d[i] = dot2(kb[i][1], qb[1], t);
  }
#define TSTEP(N, CTRL, BIT)                                                                    \
  _Pragma("unroll") for (int i = 0; i < N; ++i) {                                              \
    const bool hb = (c & BIT) != 0;                                                            \
    const float keep = hb ? d[i + N] : d[i], send = hb ? d[i] : d[i + N];                      \
    d[i] = keep + dpp<CTRL>(send);                                                             \
  }
  TSTEP(8, XOR8, 8) TSTEP(4, XOR7, 4) TSTEP(2, XOR2, 2) TSTEP(1, XOR1, 1)
#undef TSTEP
  const float sc = rbf(rbf(d[0]) * 0.07216878364870322f) * 1.4426950408889634f;
  const float mw = wave_max(sc);
  const float p = __builtin_amdgcn_exp2f(sc - mw);
  const float lw = wave_sum(p);
  S.pl[wave][lane] = p;
  if (lane == 0) { S.ml[wave][0] = mw; S.ml[wave][1] = lw; }

  const int occ = tid % 80, org = tid / 80;
  u32x4 wor[32];
  const uint16_t* wop = P.wo + (size_t)(h * DH + (org < 6 ? org : 5) * 32) * D + occ * 8;
  __builtin_amdgcn_sched_barrier(0);
#pragma unroll
  for (int r = 0; r < 16; ++r) wor[r] = *(const u32x4*)(wop + (size_t)r * D);
  const int fcol = xcd * 192 + idx * 6;
  u32 g0[3], g1[3], u0[3], u1[3];
  u32x4 wdr;
  if (tid < 320) {
    const u32* a = (const u32*)(P.wg + (size_t)(2 * tid) * F + fcol);
    const u32* a1 = (const u32*)(P.wg + (size_t)(2 * tid + 1) * F + fcol);
    const u32* bq = (const u32*)(P.wu + (size_t)(2 * tid) * F + fcol);
    const u32* b1 = (const u32*)(P.wu + (size_t)(2 * tid + 1) * F + fcol);
#pragma unroll
    for (int k = 0; k < 3; ++k) { g0[k] = a[k]; g1[k] = a1[k]; u0[k] = bq[k]; u1[k] = b1[k]; }
  }
  if (tid < 480) wdr = *(const u32x4*)(P.wd + (size_t)(fcol + org) * D + occ * 8);
  u32 xres = 0;
  if (wg < NRED && tid < 4) xres = ((const u32*)(P.x + (tid >> 1) * D + wg * 4))[tid & 1];

  asm volatile("s_waitcnt lgkmcnt(0)" ::: "memory");
  float acc[12];
#pragma unroll
  for (int k = 0; k < 12; ++k) acc[k] = 0.f;
#pragma unroll
  for (int i4 = 0; i4 < 4; ++i4) {
    if (i4 == 2) {
      __builtin_amdgcn_sched_barrier(0);
#pragma unroll
      for (int r = 16; r < 32; ++r) wor[r] = *(const u32x4*)(wop + (size_t)r * D);
      __builtin_amdgcn_sched_barrier(0);
    }
    const f32x4 pp = *(const f32x4*)&S.pl[wave][r4 * 16 + i4 * 4];
#pragma unroll
    for (int j = 0; j < 4; ++j) {
      const u32x4 v = va[i4 * 4 + j]; const u32x2 w = vb[i4 * 4 + j];
#pragma unroll
      for (int k = 0; k < 4; ++k) {
        acc[2 * k] = fmaf(pp[j], lo2f(v[k]), acc[2 * k]);
        acc[2 * k + 1] = fmaf(pp[j], hi2f(v[k]), acc[2 * k + 1]);
      }
#pragma unroll
      for (int k = 0; k < 2; ++k) {
        acc[8 + 2 * k] = fmaf(pp[j], lo2f(w[k]), acc[8 + 2 * k]);
        acc[8 + 2 * k + 1] = fmaf(pp[j], hi2f(w[k]), acc[8 + 2 * k + 1]);
      }
    }
  }
#pragma unroll
  for (int k = 0; k < 6; ++k) acc[k] = sw32sum(acc[k], acc[k + 6]);
  {
    float* dst = &S.red[wave * 2 + (r4 & 1)][0];
    if (half == 0) {
      *(f32x4*)&dst[8 * c16] = f32x4{acc[0], acc[1], acc[2], acc[3]};
      *(f32x2*)&dst[8 * c16 + 4] = f32x2{acc[4], acc[5]};
    } else {
      *(f32x2*)&dst[8 * c16 + 6] = f32x2{acc[0], acc[1]};
      *(f32x4*)&dst[128 + 4 * c16] = f32x4{acc[2], acc[3], acc[4], acc[5]};
    }
  }
  __syncthreads();
  float Mwg, Lwg;
  {
    float mm[8];
    Mwg = -INFINITY;
#pragma unroll
    for (int w = 0; w < 8; ++w) { mm[w] = S.ml[w][0]; Mwg = fmaxf(Mwg, mm[w]); }
    Lwg = 0.f;
#pragma unroll
    for (int w = 0; w < 8; ++w) { mm[w] = __builtin_amdgcn_exp2f(mm[w] - Mwg); Lwg = fmaf(mm[w], S.ml[w][1], Lwg); }
    if (tid < 96) {
      float o0 = 0.f, o1 = 0.f;
#pragma unroll
      for (int w = 0; w < 8; ++w) {
        const f32x2 a = *(const f32x2*)&S.red[2 * w][2 * tid], bb = *(const f32x2*)&S.red[2 * w + 1][2 * tid];
        o0 = fmaf(mm[w], a[0] + bb[0], o0); o1 = fmaf(mm[w], a[1] + bb[1], o1);
      }
      S.opk[tid] = pk2(o0, o1);
    }
  }
  __syncthreads();
  const u32 ta = (u32)ep + 1u, tb = (u32)(ep >> 32) + 1u;
  if (tid < 480) {
    float a8[8];
#pragma unroll
    for (int k = 0; k < 8; ++k) a8[k] = 0.f;
#pragma unroll
    for (int j4 = 0; j4 < 4; ++j4) {
      const u32x4 op = *(const u32x4*)&S.opk[org * 16 + j4 * 4];
#pragma unroll
      for (int jj = 0; jj < 4; ++jj) {
        const u32x4 w0 = wor[(j4 * 4 + jj) * 2], w1 = wor[(j4 * 4 + jj) * 2 + 1];
#pragma unroll
        for (int k = 0; k < 4; ++k) {
          a8[2 * k] = dot2(__builtin_amdgcn_perm(w1[k], w0[k], 0x05040100), op[jj], a8[2 * k]);
          a8[2 * k + 1] = dot2(__builtin_amdgcn_perm(w1[k], w0[k], 0x07060302), op[jj], a8[2 * k + 1]);
        }
      }
    }
    *(f32x4*)&S.ypart[org][occ * 8] = f32x4{a8[0], a8[1], a8[2], a8[3]};
    *(f32x4*)&S.ypart[org][occ * 8 + 4] = f32x4{a8[4], a8[5], a8[6], a8[7]};
  }
  __syncthreads();
  if (tid < 320) {
    float y0 = 0.f, y1 = 0.f;
#pragma unroll
    for (int g = 0; g < 6; ++g) { const f32x2 t = *(const f32x2*)&S.ypart[g][2 * tid]; y0 += t[0]; y1 += t[1]; }
    st_gran(P.ws + WS_Y + ((size_t)((tid >> 1) * 256 + prod) * 2 + (tid & 1)) * 16, y0, y1, ta, tb);
  } else if (tid == 320) {
    st_gran(P.ws + WS_ML + (size_t)prod * 16, Mwg, Lwg, ta, tb);
  }
  float hres0 = 0.f, hres1 = 0.f;
  if (wg < NRED) {
    const unsigned char* py = P.ws + WS_Y + ((size_t)wg * 512 + tid) * 16;
    const unsigned char* pm = P.ws + WS_ML + (size_t)(tid >> 1) * 16;
    u32x4 gy, gm;
    for (int it = 0; it < SPIN_LIMIT; ++it) {
      ld_gran2(py, pm, gy, gm);
      const bool ok = gy[1] == ta && gy[3] == tb && gm[1] == ta && gm[3] == tb;
      if (__all(ok)) break;
      __builtin_amdgcn_s_sleep(1);
    }
    const float m = __uint_as_float(gm[0]), l = __uint_as_float(gm[2]);
    float M = fmaxf(m, dpp<XOR2>(m)); M = fmaxf(M, dpp<XOR7>(M));
    const float e = __builtin_amdgcn_exp2f(m - M);
    float L = e * l; L += dpp<XOR2>(L); L += dpp<XOR7>(L);
    const float wgt = e / L;
    float y0 = wgt * __uint_as_float(gy[0]), y1 = wgt * __uint_as_float(gy[2]);
    y0 += dpp<XOR2>(y0); y1 += dpp<XOR2>(y1);
    {
      const float a0 = dpp<XOR1>(dpp<XOR7>(y0)), a1 = dpp<XOR1>(dpp<XOR7>(y1));
      y0 += a0; y1 += a1;
    }
    y0 += dpp<XOR8>(y0); y1 += dpp<XOR8>(y1);
    y0 = sw16sum(y0, y0); y1 = sw16sum(y1, y1);
    y0 = sw32sum(y0, y0); y1 = sw32sum(y1, y1);
    if (lane < 2) { S.wsum[wave][lane * 2] = y0; S.wsum[wave][lane * 2 + 1] = y1; }
    __syncthreads();
    if (tid < 4) {
      const int bb = tid >> 1, g = tid & 1;
      float a0 = 0.f, a1 = 0.f;
#pragma unroll
      for (int w = 0; w < 4; ++w) { a0 += S.wsum[bb * 4 + w][g * 2]; a1 += S.wsum[bb * 4 + w][g * 2 + 1]; }
      hres0 = rbf(lo2f(xres) + rbf(a0)); hres1 = rbf(hi2f(xres) + rbf(a1));
      st_gran(P.ws + WS_H + (size_t)(bb * 320 + wg * 2 + g) * 16, hres0, hres1, ta, tb);
    }
  }
  float h00 = 0.f, h01 = 0.f, h10 = 0.f, h11 = 0.f;
  if (tid < 320) {
    const unsigned char* p0 = P.ws + WS_H + (size_t)tid * 16;
    const unsigned char* p1 = p0 + 320 * 16;
    u32x4 ga, gb;
    for (int it = 0; it < SPIN_LIMIT; ++it) {
      ld_gran2(p0, p1, ga, gb);
      const bool ok = ga[1] == ta && ga[3] == tb && gb[1] == ta && gb[3] == tb;
      if (__all(ok)) break;
      __builtin_amdgcn_s_sleep(1);
    }
    h00 = __uint_as_float(ga[0]); h01 = __uint_as_float(ga[2]); h10 = __uint_as_float(gb[0]); h11 = __uint_as_float(gb[2]);
    *(f32x2*)&S.hs[0][2 * tid] = f32x2{h00, h01};
    *(f32x2*)&S.hs[1][2 * tid] = f32x2{h10, h11};
  }
  __syncthreads();
  if (tid < 320) {
    u32 hp[2];
#pragma unroll
    for (int bb = 0; bb < 2; ++bb) {
      float xv[10];
#pragma unroll
      for (int i = 0; i < 5; ++i) { const f32x2 t = *(const f32x2*)&S.hs[bb][lane * 10 + 2 * i]; xv[2 * i] = t[0]; xv[2 * i + 1] = t[1]; }
      float s = 0.f;
#pragma unroll
      for (int i = 0; i < 10; ++i) s += xv[i];
      const float mean = wave_sum(s) * (1.f / D);
      float q = 0.f;
#pragma unroll
      for (int i = 0; i < 10; ++i) { const float dd = xv[i] - mean; q += dd * dd; }
      const float var = wave_sum(q) * (1.f / D);
      const float mb = rbf(mean), den = rbf(sqrtf(rbf(rbf(var) + 1e-5f)));
      const float a = bb ? h10 : h00, bq = bb ? h11 : h01;
      hp[bb] = pk2(rbf(a - mb) / den, rbf(bq - mb) / den);
    }
    float r[32];
#pragma unroll
    for (int k = 0; k < 3; ++k) {
      const u32 gl = __builtin_amdgcn_perm(g1[k], g0[k], 0x05040100), gh = __builtin_amdgcn_perm(g1[k], g0[k], 0x07060302);
      const u32 ul = __builtin_amdgcn_perm(u1[k], u0[k], 0x05040100), uh = __builtin_amdgcn_perm(u1[k], u0[k], 0x07060302);
#pragma unroll
      for (int bb = 0; bb < 2; ++bb) {
        r[(0 + bb) * 8 + 2 * k] = dot2(gl, hp[bb], 0.f); r[(0 + bb) * 8 + 2 * k + 1] = dot2(gh, hp[bb], 0.f);
        r[(2 + bb) * 8 + 2 * k] = dot2(ul, hp[bb], 0.f); r[(2 + bb) * 8 + 2 * k + 1] = dot2(uh, hp[bb], 0.f);
      }
    }
#pragma unroll
    for (int q4 = 0; q4 < 4; ++q4) { r[q4 * 8 + 6] = 0.f; r[q4 * 8 + 7] = 0.f; }
#pragma unroll
    for (int i = 0; i < 16; ++i) r[i] = sw16sum(r[i], r[i + 16]);
#define TSTEP(N, CTRL, BIT)                                                                    \
  _Pragma("unroll") for (int i = 0; i < N; ++i) {                                              \
    const bool hb = (c & BIT) != 0;                                                            \
    const float keep = hb ? r[i + N] : r[i], send = hb ? r[i] : r[i + N];                      \
    r[i] = keep + dpp<CTRL>(send);                                                             \
  }
    TSTEP(8, XOR8, 8) TSTEP(4, XOR7, 4) TSTEP(2, XOR2, 2) TSTEP(1, XOR1, 1)
#undef TSTEP
    r[0] = sw32sum(r[0], r[0]);
    if (lane < 32) S.red[wave][lane] = r[0];
  }
  __syncthreads();
  if (tid < 16) {
    float sg = 0.f, su = 0.f;
#pragma unroll
    for (int w = 0; w < 5; ++w) { sg += S.red[w][tid]; su += S.red[w][16 + tid]; }
    sg = rbf(sg); su = rbf(su);
    const float t = 0.7978845608028654f * (sg + 0.044715f * sg * sg * sg);
    const float ge = rbf(0.5f * sg * (1.f + tanhf(t)));
    S.gv[tid >> 3][tid & 7] = rbf(ge * su);
  }
  __syncthreads();
  if (tid < 480) {
#pragma unroll
    for (int bb = 0; bb < 2; ++bb) {
      const float gg = S.gv[bb][org];
      *(f32x4*)&S.zpart[org][bb][occ * 8] = f32x4{gg * lo2f(wdr[0]), gg * hi2f(wdr[0]), gg * lo2f(wdr[1]), gg * hi2f(wdr[1])};
      *(f32x4*)&S.zpart[org][bb][occ * 8 + 4] = f32x4{gg * lo2f(wdr[2]), gg * hi2f(wdr[2]), gg * lo2f(wdr[3]), gg * hi2f(wdr[3])};
    }
  }
  __syncthreads();
  if (tid < 320) {
#pragma unroll
    for (int bb = 0; bb < 2; ++bb) {
      float z0 = 0.f, z1 = 0.f;
#pragma unroll
      for (int g = 0; g < 6; ++g) { const f32x2 t = *(const f32x2*)&S.zpart[g][bb][2 * tid]; z0 += t[0]; z1 += t[1]; }
      st_gran(P.ws + WS_Z + ((size_t)((tid >> 1) * 256 + wg) * 4 + bb * 2 + (tid & 1)) * 16, z0, z1, ta, tb);
    }
  }
  if (wg < NRED) {
    const unsigned char* p0 = P.ws + WS_Z + ((size_t)wg * 1024 + tid) * 16;
    const unsigned char* p1 = p0 + 512 * 16;
    u32x4 ga, gb;
    for (int it = 0; it < SPIN_LIMIT; ++it) {
      ld_gran2(p0, p1, ga, gb);
      const bool ok = ga[1] == ta && ga[3] == tb && gb[1] == ta && gb[3] == tb;
      if (__all(ok)) break;
      __builtin_amdgcn_s_sleep(1);
    }
    float z0 = __uint_as_float(ga[0]) + __uint_as_float(gb[0]), z1 = __uint_as_float(ga[2]) + __uint_as_float(gb[2]);
    {
      const float a0 = dpp<XOR1>(dpp<XOR2>(dpp<XOR7>(z0))), a1 = dpp<XOR1>(dpp<XOR2>(dpp<XOR7>(z1)));
      z0 += a0; z1 += a1;
    }
    z0 += dpp<XOR8>(z0); z1 += dpp<XOR8>(z1);
    z0 = sw16sum(z0, z0); z1 = sw16sum(z1, z1);
    z0 = sw32sum(z0, z0); z1 = sw32sum(z1, z1);
    if (lane < 4) { S.fin[wave][lane * 2] = z0; S.fin[wave][lane * 2 + 1] = z1; }
    __syncthreads();
    if (tid < 4) {
      float a0 = 0.f, a1 = 0.f;
#pragma unroll
      for (int w = 0; w < 8; ++w) { a0 += S.fin[w][tid * 2]; a1 += S.fin[w][tid * 2 + 1]; }
      const u32 o = pk2(hres0 + rbf(a0), hres1 + rbf(a1));
      ((u32*)(P.out + (tid >> 1) * D + wg * 4))[tid & 1] = o;
    }
    if (wg == 0 && tid == 0) {
      u32x2 ne = {ta, tb};
      asm volatile("global_store_dwordx2 %0, %1, off sc1" :: "v"(P.ws + WS_EPOCH), "v"(ne) : "memory");
    }
  }
}
}

extern "C" void kernel_launch(void* const* d_in, const int* in_sizes, int n_in, void* d_out, int out_size,
                              void* d_ws, size_t ws_size, hipStream_t stream) {
  Params P;
  P.x = (const uint16_t*)d_in[0]; P.kc = (const uint16_t*)d_in[1]; P.vc = (const uint16_t*)d_in[2];
  P.wq = (const uint16_t*)d_in[3]; P.wo = (const uint16_t*)d_in[4];
  P.wg = (const uint16_t*)d_in[6]; P.wu = (const uint16_t*)d_in[7]; P.wd = (const uint16_t*)d_in[8];
  P.out = (uint16_t*)d_out; P.ws = (unsigned char*)d_ws;
  fused_kernel<<<NWG, NT, 0, stream>>>(P);
}
```

```cpp
#include <hip/hip_runtime.h>
#include <hip/hip_bf16.h>
#include <stdint.h>

namespace {
typedef unsigned u32;
typedef u32 u32x2 __attribute__((ext_vector_type(2)));
typedef u32 u32x4 __attribute__((ext_vector_type(4)));
typedef float f32x2 __attribute__((ext_vector_type(2)));
typedef float f32x4 __attribute__((ext_vector_type(4)));
typedef __bf16 bf16x2 __attribute__((ext_vector_type(2)));

constexpr int D = 640, H = 32, SK = 8192, DH = 192, W = 2048, F = 1536, QN = H * DH;
constexpr int NWG = 256, NT = 768, NRED = 80;
#define PACED 0
#define PWIN 3
#define GUAUX 2
constexpr int SPIN_LIMIT = 1 << 20;

constexpr size_t WS_EPOCH = 0;
constexpr size_t WS_Q = 4096;
constexpr size_t WS_O = WS_Q + (size_t)32 * 2 * 480 * 16;
constexpr size_t WS_H = WS_O + (size_t)256 * 128 * 16;
constexpr size_t WS_Y = WS_H + 640 * 16;
constexpr size_t WS_Z = WS_Y + (size_t)192 * 640 * 16;
constexpr size_t WS_END = WS_Z + (size_t)64 * 640 * 16;

__device__ __forceinline__ float rbf(float f) { return __bfloat162float(__float2bfloat16(f)); }
__device__ __forceinline__ float lo2f(u32 v) { return __uint_as_float(v << 16); }
__device__ __forceinline__ float hi2f(u32 v) { return __uint_as_float(v & 0xffff0000u); }
__device__ __forceinline__ u32 pk2(float lo, float hi) {
  u32 r; asm("v_cvt_pk_bf16_f32 %0, %1, %2" : "=v"(r) : "v"(lo), "v"(hi)); return r;
}
__device__ __forceinline__ float dot2(u32 a, u32 b, float c) {
  return __builtin_amdgcn_fdot2_f32_bf16(__builtin_bit_cast(bf16x2, a), __builtin_bit_cast(bf16x2, b), c, false);
}
__device__ __forceinline__ u32 plo(u32 w0, u32 w1) { return __builtin_amdgcn_perm(w1, w0, 0x05040100); }
__device__ __forceinline__ u32 phi(u32 w0, u32 w1) { return __builtin_amdgcn_perm(w1, w0, 0x07060302); }
template <int CTRL> __device__ __forceinline__ float dpp(float x) {
  return __builtin_bit_cast(float, __builtin_amdgcn_mov_dpp(__builtin_bit_cast(int, x), CTRL, 0xf, 0xf, true));
}
constexpr int XOR1 = 0xB1, XOR2 = 0x4E, XOR7 = 0x141, XOR8 = 0x128;
__device__ __forceinline__ float sw16sum(float a, float b) {
  auto s = __builtin_amdgcn_permlane16_swap(__float_as_uint(a), __float_as_uint(b), false, false);
  return __uint_as_float(s[0]) + __uint_as_float(s[1]);
}
__device__ __forceinline__ float sw32sum(float a, float b) {
  auto s = __builtin_amdgcn_permlane32_swap(__float_as_uint(a), __float_as_uint(b), false, false);
  return __uint_as_float(s[0]) + __uint_as_float(s[1]);
}
__device__ __forceinline__ float upper32(float a) {
  auto s = __builtin_amdgcn_permlane32_swap(__float_as_uint(a), __float_as_uint(a), false, false);
  return __uint_as_float(s[1]);
}
__device__ __forceinline__ float x1632max(float x) {
  auto s = __builtin_amdgcn_permlane16_swap(__float_as_uint(x), __float_as_uint(x), false, false);
  x = fmaxf(__uint_as_float(s[0]), __uint_as_float(s[1]));
  auto t = __builtin_amdgcn_permlane32_swap(__float_as_uint(x), __float_as_uint(x), false, false);
  return fmaxf(__uint_as_float(t[0]), __uint_as_float(t[1]));
}
__device__ __forceinline__ float x1632sum(float x) { x = sw16sum(x, x); return sw32sum(x, x); }
__device__ __forceinline__ float row16sum(float x) {
  x += dpp<XOR1>(x); x += dpp<XOR2>(x); x += dpp<XOR7>(x); x += dpp<XOR8>(x); return x;
}
__device__ __forceinline__ float wave_sum(float x) { return x1632sum(row16sum(x)); }
__device__ __forceinline__ float wave_max(float x) {
  x = fmaxf(x, dpp<XOR1>(x)); x = fmaxf(x, dpp<XOR2>(x)); x = fmaxf(x, dpp<XOR7>(x)); x = fmaxf(x, dpp<XOR8>(x));
  return x1632max(x);
}
__device__ __forceinline__ void st_gran(unsigned char* p, float v0, float v1, u32 ta, u32 tb) {
  u32x4 g = {__float_as_uint(v0), ta, __float_as_uint(v1), tb};
  asm volatile("global_store_dwordx4 %0, %1, off sc1\n\ts_nop 1" :: "v"(p), "v"(g) : "memory");
}
__device__ __forceinline__ void ld_gran1(const unsigned char* p, u32x4& a) {
  asm volatile("global_load_dwordx4 %0, %1, off sc1\n\ts_waitcnt vmcnt(0)" : "=&v"(a) : "v"(p) : "memory");
}
__device__ __forceinline__ void ld_gran2(const unsigned char* p, const unsigned char* q, u32x4& a, u32x4& b) {
  asm volatile("global_load_dwordx4 %0, %2, off sc1\n\tglobal_load_dwordx4 %1, %3, off sc1\n\ts_waitcnt vmcnt(0)"
               : "=&v"(a), "=&v"(b) : "v"(p), "v"(q) : "memory");
}
__device__ __forceinline__ void ld_gran3(const unsigned char* p0, const unsigned char* p1, const unsigned char* p2,
                                         u32x4& a, u32x4& b, u32x4& c) {
  asm volatile("global_load_dwordx4 %0, %3, off sc1\n\tglobal_load_dwordx4 %1, %4, off sc1\n\t"
               "global_load_dwordx4 %2, %5, off sc1\n\ts_waitcnt vmcnt(0)"
               : "=&v"(a), "=&v"(b), "=&v"(c) : "v"(p0), "v"(p1), "v"(p2) : "memory");
}
__device__ __forceinline__ void ld_gran10(const unsigned char* p0, const unsigned char* p0b, const unsigned char* p1,
                                          const unsigned char* p1b, u32x4 (&g)[10]) {
  asm volatile(
      "global_load_dwordx4 %0, %10, off sc1\n\tglobal_load_dwordx4 %1, %10, off offset:1536 sc1\n\t"
      "global_load_dwordx4 %2, %10, off offset:3072 sc1\n\tglobal_load_dwordx4 %3, %11, off sc1\n\t"
      "global_load_dwordx4 %4, %11, off offset:1536 sc1\n\t"
      "global_load_dwordx4 %5, %12, off sc1\n\tglobal_load_dwordx4 %6, %12, off offset:1536 sc1\n\t"
      "global_load_dwordx4 %7, %12, off offset:3072 sc1\n\tglobal_load_dwordx4 %8, %13, off sc1\n\t"
      "global_load_dwordx4 %9, %13, off offset:1536 sc1\n\ts_waitcnt vmcnt(0)"
      : "=&v"(g[0]), "=&v"(g[1]), "=&v"(g[2]), "=&v"(g[3]), "=&v"(g[4]), "=&v"(g[5]), "=&v"(g[6]), "=&v"(g[7]),
        "=&v"(g[8]), "=&v"(g[9])
      : "v"(p0), "v"(p0b), "v"(p1), "v"(p1b) : "memory");
}
__device__ __forceinline__ __amdgpu_buffer_rsrc_t mkrsrc(const void* p, u32 bytes) {
  return __builtin_amdgcn_make_buffer_rsrc(const_cast<void*>(p), 0, bytes, 0x00020000);
}
#define TAGOK(g) ((g)[1] == ta && (g)[3] == tb)
#define LDS_LD(p) __hip_atomic_load((p), __ATOMIC_RELAXED, __HIP_MEMORY_SCOPE_WORKGROUP)

#define STAMPT(t, i) do { } while (0)
#define STAMP(i) STAMPT(0, i)
struct Params {
  const uint16_t* x; const uint16_t* kc; const uint16_t* vc; const uint16_t* wq; const uint16_t* wo;
  const uint16_t* wg; const uint16_t* wu; const uint16_t* wd; uint16_t* out; unsigned char* ws;
};

constexpr int GUS = 324;
struct Lds {
  u32 wst[48 * GUS + 12 * 640];
  u32 xnp[2][320];
  float qpart[8][2][192];
  u32 qpk[96];
  float pl[9][64];
  float ml[10][2];
  float red[18][192];
  u32 opk[2][16];
  float wsum[8][16];
  float st[5][4];
  float hres[16];
  u32 hp[2][320];
  u32 gp[2][12];
  u32 qcnt, qflag, ocnt, pad1;
};

__global__ void __launch_bounds__(NT, 3) fused_kernel(Params P) {
  __shared__ __attribute__((aligned(16))) Lds S;
  const int tid = threadIdx.x, lane = tid & 63, wave = __builtin_amdgcn_readfirstlane(tid >> 6);
  const int half = lane >> 5, c = lane & 31;
  const int r4 = lane >> 4, c16 = lane & 15;
  const int wg = blockIdx.x, xcd = wg & 7, idx = wg >> 3;
  const int h = (idx >> 3) * 8 + xcd, j8 = idx & 7, b = j8 >> 2, sp = j8 & 3;
  const int prod = h * 8 + j8;
  const bool isF = sp == 3, isO = !isF;
  const int jr = j8 - b;
  const bool isQ = isO && jr < 5;
  const int fidx = h * 2 + b;
  const int rid = h * 5 + jr;
  const bool isR = isQ && rid < NRED;

  STAMP(0);
  if (tid == 0) { S.qcnt = 0u; S.qflag = 0u; S.ocnt = 0u; }
  __syncthreads();
  const unsigned long long ep = __hip_atomic_load((const unsigned long long*)(P.ws + WS_EPOCH), __ATOMIC_RELAXED, __HIP_MEMORY_SCOPE_AGENT);
  const u32 ta = (u32)ep + 1u, tb = (u32)(ep >> 32) + 1u;
  const u32 xres = ((const u32*)(P.x + ((tid >> 2) & 1) * D + (rid % NRED) * 8))[tid & 3];
  auto stream_body = [&](const int slotw, const int ng, const int row0, const bool waitq) {
    const size_t kvoff = ((size_t)(b * H + h) * SK + (SK - W) + sp * 544 + row0) * DH;
    const __amdgpu_buffer_rsrc_t krs = mkrsrc(P.kc + kvoff, ng * 1536);
    const __amdgpu_buffer_rsrc_t vrs = mkrsrc(P.vc + kvoff, ng * 1536);
    const u32 loA = r4 * 384 + c16 * 16, loB = r4 * 384 + 256 + c16 * 8;
    u32x4 ka[16]; u32x2 kb[16];
#pragma unroll
    for (int i = 0; i < 16; ++i) {
      ka[i] = __builtin_amdgcn_raw_buffer_load_b128(krs, loA, i * 1536, 2);
      kb[i] = __builtin_amdgcn_raw_buffer_load_b64(krs, loB, i * 1536, 2);
    }
    u32x4 va[16]; u32x2 vb[16];
#pragma unroll
    for (int i = 0; i < 8; ++i) {
      va[i] = __builtin_amdgcn_raw_buffer_load_b128(vrs, loA, i * 1536, 2);
      vb[i] = __builtin_amdgcn_raw_buffer_load_b64(vrs, loB, i * 1536, 2);
    }
    __builtin_amdgcn_sched_barrier(0);
    STAMP(1);
    if (waitq) { while (LDS_LD(&S.qflag) == 0u) __builtin_amdgcn_s_sleep(1); }
    asm volatile("" ::: "memory");
    STAMP(2);
    const u32x4 qa = *(const u32x4*)&S.qpk[4 * c16];
    const u32x2 qb = *(const u32x2*)&S.qpk[64 + 2 * c16];
    float d[16];
#pragma unroll
    for (int i = 0; i < 16; ++i) {
      float t = dot2(ka[i][0], qa[0], 0.f);
      t = dot2(ka[i][1], qa[1], t); t = dot2(ka[i][2], qa[2], t); t = dot2(ka[i][3], qa[3], t);
      t = dot2(kb[i][0], qb[0], t); d[i] = dot2(kb[i][1], qb[1], t);
    }
    __builtin_amdgcn_sched_barrier(0);
#pragma unroll
    for (int i = 8; i < 16; ++i) {
      va[i] = __builtin_amdgcn_raw_buffer_load_b128(vrs, loA, i * 1536, 2);
      vb[i] = __builtin_amdgcn_raw_buffer_load_b64(vrs, loB, i * 1536, 2);
    }
    __builtin_amdgcn_sched_barrier(0);
#define TSTEP(A, N, CTRL, BIT)                                                                 \
  _Pragma("unroll") for (int i = 0; i < N; ++i) {                                              \
    const bool hb = (c & BIT) != 0;                                                            \
    const float keep = hb ? A[i + N] : A[i], send = hb ? A[i] : A[i + N];                      \
    A[i] = keep + dpp<CTRL>(send);                                                             \
  }
    TSTEP(d, 8, XOR8, 8) TSTEP(d, 4, XOR7, 4) TSTEP(d, 2, XOR2, 2) TSTEP(d, 1, XOR1, 1)
    const float sc = c16 < ng ? rbf(rbf(d[0]) * 0.07216878364870322f) * 1.4426950408889634f : -INFINITY;
    const float mw = wave_max(sc);
    const float p = __builtin_amdgcn_exp2f(sc - mw);
    const float lw = wave_sum(p);
    S.pl[slotw][lane] = p;
    if (lane == 0) { S.ml[slotw][0] = mw; S.ml[slotw][1] = lw; }
    STAMP(3);
    asm volatile("s_waitcnt lgkmcnt(0)" ::: "memory");
    float acc[12];
#pragma unroll
    for (int k = 0; k < 12; ++k) acc[k] = 0.f;
#pragma unroll
    for (int i4 = 0; i4 < 4; ++i4) {
      const f32x4 pp = *(const f32x4*)&S.pl[slotw][r4 * 16 + i4 * 4];
#pragma unroll
      for (int j = 0; j < 4; ++j) {
        const u32x4 v = va[i4 * 4 + j]; const u32x2 w = vb[i4 * 4 + j];
#pragma unroll
        for (int k = 0; k < 4; ++k) {
          acc[2 * k] = fmaf(pp[j], lo2f(v[k]), acc[2 * k]);
          acc[2 * k + 1] = fmaf(pp[j], hi2f(v[k]), acc[2 * k + 1]);
        }
#pragma unroll
        for (int k = 0; k < 2; ++k) {
          acc[8 + 2 * k] = fmaf(pp[j], lo2f(w[k]), acc[8 + 2 * k]);
          acc[8 + 2 * k + 1] = fmaf(pp[j], hi2f(w[k]), acc[8 + 2 * k + 1]);
        }
      }
    }
#pragma unroll
    for (int k = 0; k < 6; ++k) acc[k] = sw32sum(acc[k], acc[k + 6]);
    float* dst = &S.red[slotw * 2 + (r4 & 1)][0];
    if (half == 0) {
      *(f32x4*)&dst[8 * c16] = f32x4{acc[0], acc[1], acc[2], acc[3]};
      *(f32x2*)&dst[8 * c16 + 4] = f32x2{acc[4], acc[5]};
    } else {
      *(f32x2*)&dst[8 * c16 + 6] = f32x2{acc[0], acc[1]};
      *(f32x4*)&dst[128 + 4 * c16] = f32x4{acc[2], acc[3], acc[4], acc[5]};
    }
    STAMP(4); STAMPT(448, 19); STAMPT(256, 20); STAMPT(704, 18);
  };

  if (wave == 11 && isO) {
    stream_body(8, 8, 512, true);
  } else if (wave >= 8) {
    __builtin_amdgcn_s_setprio(3);
    const int st = tid - 512;
    if (isQ) {
      u32 xr[2][5];
#pragma unroll
      for (int bb = 0; bb < 2; ++bb) {
        const u32* xp = (const u32*)(P.x + bb * D) + lane * 5;
#pragma unroll
        for (int i = 0; i < 5; ++i) xr[bb][i] = xp[i];
      }
      const int s2 = st < 192 ? st : 191, cc = s2 % 24, slot = s2 / 24;
      u32x4 wqr[16];
      {
        const __amdgpu_buffer_rsrc_t rs = mkrsrc(P.wq + (size_t)(jr * 128) * QN + h * DH, 128 * QN * 2);
        const u32 vo = (u32)(slot * 16) * (QN * 2) + cc * 16;
#pragma unroll
        for (int i = 0; i < 16; ++i) wqr[i] = __builtin_amdgcn_raw_buffer_load_b128(rs, vo, i * QN * 2, 2);
      }
#pragma unroll
      for (int bb = 0; bb < 2; ++bb) {
        float xv[10];
#pragma unroll
        for (int i = 0; i < 5; ++i) { xv[2 * i] = lo2f(xr[bb][i]); xv[2 * i + 1] = hi2f(xr[bb][i]); }
        float s = 0.f;
#pragma unroll
        for (int i = 0; i < 10; ++i) s += xv[i];
        const float mean = wave_sum(s) * (1.f / D);
        float q = 0.f;
#pragma unroll
        for (int i = 0; i < 10; ++i) { const float d = xv[i] - mean; q += d * d; }
        const float var = wave_sum(q) * (1.f / D);
        const float mb = rbf(mean), den = rbf(sqrtf(rbf(rbf(var) + 1e-5f)));
#pragma unroll
        for (int i = 0; i < 5; ++i) S.xnp[bb][lane * 5 + i] = pk2(rbf(xv[2 * i] - mb) / den, rbf(xv[2 * i + 1] - mb) / den);
      }
      asm volatile("s_waitcnt lgkmcnt(0)" ::: "memory");
      float a[2][8];
#pragma unroll
      for (int k = 0; k < 8; ++k) { a[0][k] = 0.f; a[1][k] = 0.f; }
#pragma unroll
      for (int i4 = 0; i4 < 2; ++i4) {
        const u32x4 x0 = *(const u32x4*)&S.xnp[0][jr * 64 + slot * 8 + i4 * 4];
        const u32x4 x1 = *(const u32x4*)&S.xnp[1][jr * 64 + slot * 8 + i4 * 4];
#pragma unroll
        for (int i = 0; i < 4; ++i) {
          const u32x4 w0 = wqr[(i4 * 4 + i) * 2], w1 = wqr[(i4 * 4 + i) * 2 + 1];
#pragma unroll
          for (int k = 0; k < 4; ++k) {
            const u32 wl = plo(w0[k], w1[k]), wh = phi(w0[k], w1[k]);
            a[0][2 * k] = dot2(wl, x0[i], a[0][2 * k]); a[0][2 * k + 1] = dot2(wh, x0[i], a[0][2 * k + 1]);
            a[1][2 * k] = dot2(wl, x1[i], a[1][2 * k]); a[1][2 * k + 1] = dot2(wh, x1[i], a[1][2 * k + 1]);
          }
        }
      }
      if (st < 192) {
#pragma unroll
        for (int bb = 0; bb < 2; ++bb) {
          *(f32x4*)&S.qpart[slot][bb][cc * 8] = f32x4{a[bb][0], a[bb][1], a[bb][2], a[bb][3]};
          *(f32x4*)&S.qpart[slot][bb][cc * 8 + 4] = f32x4{a[bb][4], a[bb][5], a[bb][6], a[bb][7]};
        }
      }
      asm volatile("s_waitcnt lgkmcnt(0)" ::: "memory");
      if (lane == 0) __hip_atomic_fetch_add(&S.qcnt, 1u, __ATOMIC_RELAXED, __HIP_MEMORY_SCOPE_WORKGROUP);
      while (LDS_LD(&S.qcnt) != 3u) __builtin_amdgcn_s_sleep(1);
      asm volatile("" ::: "memory");
      STAMPT(512, 14);
      if (st < 192) {
        const int bb = st / 96, k = st % 96;
        float s0 = 0.f, s1 = 0.f;
#pragma unroll
        for (int w = 0; w < 8; ++w) { const f32x2 t = *(const f32x2*)&S.qpart[w][bb][2 * k]; s0 += t[0]; s1 += t[1]; }
        st_gran(P.ws + WS_Q + ((size_t)((h * 2 + bb) * 5 + jr) * 96 + k) * 16, s0, s1, ta, tb);
      }
    }
    if (wave == 8) {
      const unsigned char* pq = P.ws + WS_Q + (size_t)(h * 2 + b) * 480 * 16;
      const unsigned char* p0 = pq + lane * 16;
      const unsigned char* p1 = pq + (64 + c) * 16;
      u32x4 g[10];
      for (int it = 0; it < SPIN_LIMIT; ++it) {
        ld_gran10(p0, p0 + 4608, p1, p1 + 4608, g);
        bool ok = true;
#pragma unroll
        for (int i = 0; i < 10; ++i) ok = ok && TAGOK(g[i]);
        if (__all(ok)) break;
        __builtin_amdgcn_s_sleep(2);
      }
      STAMPT(512, 15);
      float s0 = 0.f, s1 = 0.f, t0 = 0.f, t1 = 0.f;
#pragma unroll
      for (int i = 0; i < 5; ++i) {
        s0 += __uint_as_float(g[i][0]); s1 += __uint_as_float(g[i][2]);
        t0 += __uint_as_float(g[5 + i][0]); t1 += __uint_as_float(g[5 + i][2]);
      }
      S.qpk[lane] = pk2(s0, s1);
      if (lane < 32) S.qpk[64 + lane] = pk2(t0, t1);
      asm volatile("s_waitcnt lgkmcnt(0)" ::: "memory");
      if (lane == 0) __hip_atomic_store(&S.qflag, 1u, __ATOMIC_RELAXED, __HIP_MEMORY_SCOPE_WORKGROUP);
    } else {
      __builtin_amdgcn_s_setprio(3);
      const int tw = st - 64;
      if (isO) {
        const __amdgpu_buffer_rsrc_t ro = mkrsrc(P.wo + (size_t)(h * DH + jr * 32) * D, 32 * D * 2);
        u32x4 w0[10], w1[10];
#pragma unroll
        for (int i = 0; i < 10; ++i) {
          const int task = tw + 128 * i, k = task / 80, ch = task % 80;
          w0[i] = __builtin_amdgcn_raw_buffer_load_b128(ro, (u32)(2 * k) * (D * 2) + ch * 16, 0, 2);
          w1[i] = __builtin_amdgcn_raw_buffer_load_b128(ro, (u32)(2 * k) * (D * 2) + ch * 16, D * 2, 2);
        }
#pragma unroll
        for (int i = 0; i < 10; ++i) {
          const int task = tw + 128 * i, k = task / 80, ch = task % 80;
          u32* d = &S.wst[k * 640 + ch * 8];
          *(u32x4*)d = u32x4{plo(w0[i][0], w1[i][0]), phi(w0[i][0], w1[i][0]), plo(w0[i][1], w1[i][1]), phi(w0[i][1], w1[i][1])};
          *(u32x4*)(d + 4) = u32x4{plo(w0[i][2], w1[i][2]), phi(w0[i][2], w1[i][2]), plo(w0[i][3], w1[i][3]), phi(w0[i][3], w1[i][3])};
        }
      } else {
        {
          const __amdgpu_buffer_rsrc_t rg = mkrsrc(P.wg + fidx * 24, (D * F - fidx * 24) * 2);
          u32x4 a0[5], a1[5];
#pragma unroll
          for (int i = 0; i < 5; ++i) {
            const int task = tw + 192 * i, k = task / 3, q = task % 3;
            const u32 vo = (u32)(2 * k) * (F * 2) + q * 16;
            a0[i] = __builtin_amdgcn_raw_buffer_load_b128(rg, vo, 0, GUAUX);
            a1[i] = __builtin_amdgcn_raw_buffer_load_b128(rg, vo, F * 2, GUAUX);
          }
#pragma unroll
          for (int i = 0; i < 5; ++i) {
            const int task = tw + 192 * i, k = task / 3, q = task % 3;
#pragma unroll
            for (int e = 0; e < 4; ++e) {
              S.wst[(q * 8 + 2 * e) * GUS + k] = plo(a0[i][e], a1[i][e]);
              S.wst[(q * 8 + 2 * e + 1) * GUS + k] = phi(a0[i][e], a1[i][e]);
            }
          }
        }
        const __amdgpu_buffer_rsrc_t rd = mkrsrc(P.wd + (size_t)(fidx * 24) * D, 24 * D * 2);
        u32x4 w0[5], w1[5];
#pragma unroll
        for (int i = 0; i < 5; ++i) {
          const int task = tw + 192 * i, k = task / 80, ch = task % 80;
          w0[i] = __builtin_amdgcn_raw_buffer_load_b128(rd, (u32)(2 * k) * (D * 2) + ch * 16, 0, 2);
          w1[i] = __builtin_amdgcn_raw_buffer_load_b128(rd, (u32)(2 * k) * (D * 2) + ch * 16, D * 2, 2);
        }
#pragma unroll
        for (int i = 0; i < 5; ++i) {
          const int task = tw + 192 * i, k = task / 80, ch = task % 80;
          u32* d = &S.wst[48 * GUS + k * 640 + ch * 8];
          *(u32x4*)d = u32x4{plo(w0[i][0], w1[i][0]), phi(w0[i][0], w1[i][0]), plo(w0[i][1], w1[i][1]), phi(w0[i][1], w1[i][1])};
          *(u32x4*)(d + 4) = u32x4{plo(w0[i][2], w1[i][2]), phi(w0[i][2], w1[i][2]), plo(w0[i][3], w1[i][3]), phi(w0[i][3], w1[i][3])};
        }
      }
      STAMPT(576, 16);
    }
  } else {
    stream_body(wave, isF ? 13 : 16, isF ? wave * 52 : wave * 64, true);
  }
  __syncthreads();
  if (tid < 128) {
    float mm[9];
    float Mwg = -INFINITY;
#pragma unroll
    for (int w = 0; w < 9; ++w) { mm[w] = (w < 8 || isO) ? S.ml[w][0] : -INFINITY; Mwg = fmaxf(Mwg, mm[w]); }
    float Lwg = 0.f;
#pragma unroll
    for (int w = 0; w < 9; ++w) { mm[w] = __builtin_amdgcn_exp2f(mm[w] - Mwg); Lwg = fmaf(mm[w], (w < 8 || isO) ? S.ml[w][1] : 0.f, Lwg); }
    const int t = tid < 97 ? (tid > 0 ? tid - 1 : 0) : 95;
    float o0 = 0.f, o1 = 0.f;
#pragma unroll
    for (int w = 0; w < 9; ++w) {
      if (w < 8 || isO) {
        const f32x2 a = *(const f32x2*)&S.red[2 * w][2 * t], bb = *(const f32x2*)&S.red[2 * w + 1][2 * t];
        o0 = fmaf(mm[w], a[0] + bb[0], o0); o1 = fmaf(mm[w], a[1] + bb[1], o1);
      }
    }
    if (tid == 0) { o0 = Mwg; o1 = Lwg; }
    if (tid < 97) st_gran(P.ws + WS_O + ((size_t)prod * 128 + tid) * 16, o0, o1, ta, tb);
    if (lane == 0) __hip_atomic_fetch_add(&S.ocnt, 1u, __ATOMIC_RELAXED, __HIP_MEMORY_SCOPE_WORKGROUP);
    STAMP(5);
    if (isO) {
      const unsigned char* pb = P.ws + WS_O + (size_t)(h * 8 + wave * 4 + r4) * 128 * 16;
      u32x4 go, gm;
      for (int it = 0; it < SPIN_LIMIT; ++it) {
        ld_gran2(pb + (1 + jr * 16 + c16) * 16, pb, go, gm);
        if (__all(TAGOK(go) && TAGOK(gm))) break;
        __builtin_amdgcn_s_sleep(1);
      }
      const float m = __uint_as_float(gm[0]), l = __uint_as_float(gm[2]);
      const float M = x1632max(m);
      const float e = __builtin_amdgcn_exp2f(m - M);
      const float L = x1632sum(e * l);
      const float a0 = x1632sum(e * __uint_as_float(go[0])), a1 = x1632sum(e * __uint_as_float(go[2]));
      if (lane < 16) S.opk[wave][lane] = pk2(a0 / L, a1 / L);
    }
  }
  const int gslot = (tid >> 2) * 8 + (tid & 3);
  if (isO) {
    __syncthreads();
    STAMP(6);
    if (tid < 320) {
      float y00 = 0.f, y01 = 0.f, y10 = 0.f, y11 = 0.f;
#pragma unroll
      for (int k4 = 0; k4 < 4; ++k4) {
        const u32x4 oa = *(const u32x4*)&S.opk[0][k4 * 4], ob = *(const u32x4*)&S.opk[1][k4 * 4];
#pragma unroll
        for (int k = 0; k < 4; ++k) {
          const u32x2 w = *(const u32x2*)&S.wst[(k4 * 4 + k) * 640 + 2 * tid];
          y00 = dot2(w[0], oa[k], y00); y01 = dot2(w[1], oa[k], y01);
          y10 = dot2(w[0], ob[k], y10); y11 = dot2(w[1], ob[k], y11);
        }
      }
      unsigned char* py = P.ws + WS_Y + ((size_t)(h * 6 + jr) * 640 + gslot) * 16;
      st_gran(py, y00, y01, ta, tb);
      st_gran(py + 64, y10, y11, ta, tb);
    }
    STAMP(7);
  }
  if (isR) {
    if (tid < 512) {
      const unsigned char* p0 = P.ws + WS_Y + ((size_t)(tid >> 3) * 640 + rid * 8 + (tid & 7)) * 16;
      constexpr size_t ST = (size_t)64 * 640 * 16;
      u32x4 ga, gb, gc;
      for (int it = 0; it < SPIN_LIMIT; ++it) {
        ld_gran3(p0, p0 + ST, p0 + 2 * ST, ga, gb, gc);
        if (__all(TAGOK(ga) && TAGOK(gb) && TAGOK(gc))) break;
        __builtin_amdgcn_s_sleep(1);
      }
      STAMP(8);
      float y0 = __uint_as_float(ga[0]) + __uint_as_float(gb[0]) + __uint_as_float(gc[0]);
      float y1 = __uint_as_float(ga[2]) + __uint_as_float(gb[2]) + __uint_as_float(gc[2]);
      y0 += dpp<XOR8>(y0); y1 += dpp<XOR8>(y1);
      y0 = x1632sum(y0); y1 = x1632sum(y1);
      if (lane < 8) *(f32x2*)&S.wsum[wave][lane * 2] = f32x2{y0, y1};
    }
    __syncthreads();
    if (tid < 8) {
      float a0 = 0.f, a1 = 0.f;
#pragma unroll
      for (int w = 0; w < 8; ++w) { a0 += S.wsum[w][tid * 2]; a1 += S.wsum[w][tid * 2 + 1]; }
      const float h0 = rbf(lo2f(xres) + rbf(a0)), h1 = rbf(hi2f(xres) + rbf(a1));
      S.hres[tid * 2] = h0; S.hres[tid * 2 + 1] = h1;
      st_gran(P.ws + WS_H + (size_t)(rid * 8 + tid) * 16, h0, h1, ta, tb);
    }
    STAMP(9);
  }
  if (isF) {
    if (wave >= 8) {
      while (LDS_LD(&S.ocnt) != 2u) __builtin_amdgcn_s_sleep(1);
      asm volatile("" ::: "memory");
      const int st = tid - 512;
      const __amdgpu_buffer_rsrc_t ru = mkrsrc(P.wu + fidx * 24, (D * F - fidx * 24) * 2);
      u32x4 a0[4], a1[4];
#pragma unroll
      for (int i = 0; i < 4; ++i) {
        const int task = st + 256 * i, k = task / 3, q = task % 3;
        const u32 vo = task < 960 ? (u32)(2 * k) * (F * 2) + q * 16 : 0x7fffff00u;
        a0[i] = __builtin_amdgcn_raw_buffer_load_b128(ru, vo, 0, GUAUX);
        a1[i] = __builtin_amdgcn_raw_buffer_load_b128(ru, vo, F * 2, GUAUX);
      }
#pragma unroll
      for (int i = 0; i < 4; ++i) {
        const int task = st + 256 * i, k = task / 3, q = task % 3;
        if (task < 960) {
#pragma unroll
          for (int e = 0; e < 4; ++e) {
            S.wst[(24 + q * 8 + 2 * e) * GUS + k] = plo(a0[i][e], a1[i][e]);
            S.wst[(24 + q * 8 + 2 * e + 1) * GUS + k] = phi(a0[i][e], a1[i][e]);
          }
        }
      }
      STAMPT(512, 17);
    }
    if (tid < 320) {
      const unsigned char* p0 = P.ws + WS_H + (size_t)gslot * 16;
      u32x4 ga, gb;
      for (int it = 0; it < SPIN_LIMIT; ++it) {
        ld_gran2(p0, p0 + 64, ga, gb);
        if (__all(TAGOK(ga) && TAGOK(gb))) break;
        __builtin_amdgcn_s_sleep(1);
      }
      STAMP(10);
      const float h00 = __uint_as_float(ga[0]), h01 = __uint_as_float(ga[2]), h10 = __uint_as_float(gb[0]), h11 = __uint_as_float(gb[2]);
      S.hp[0][tid] = (ga[0] >> 16) | (ga[2] & 0xffff0000u);
      S.hp[1][tid] = (gb[0] >> 16) | (gb[2] & 0xffff0000u);
      float s0 = h00 + h01, q0 = h00 * h00 + h01 * h01, s1 = h10 + h11, q1 = h10 * h10 + h11 * h11;
      s0 = sw32sum(s0, s1); q0 = sw32sum(q0, q1);
      s0 = sw16sum(s0, q0);
      s0 = row16sum(s0);
      if (c16 == 0) S.st[wave][r4] = s0;
    }
    __syncthreads();
    {
      const u32* wrow = &S.wst[((r4 >> 1) * 24 + 2 * wave + (r4 & 1)) * GUS + 20 * c16];
      float d0 = 0.f, d1 = 0.f, cs = 0.f;
#pragma unroll
      for (int i = 0; i < 5; ++i) {
        const u32x4 w = *(const u32x4*)&wrow[4 * i];
        const u32x4 ha = *(const u32x4*)&S.hp[0][20 * c16 + 4 * i], hb = *(const u32x4*)&S.hp[1][20 * c16 + 4 * i];
#pragma unroll
        for (int k = 0; k < 4; ++k) { d0 = dot2(w[k], ha[k], d0); d1 = dot2(w[k], hb[k], d1); cs = dot2(w[k], 0x3f803f80u, cs); }
      }
      d0 = row16sum(d0); d1 = row16sum(d1); cs = row16sum(cs);
      float pre[2];
#pragma unroll
      for (int bb = 0; bb < 2; ++bb) {
        float s = 0.f, q = 0.f;
#pragma unroll
        for (int w = 0; w < 5; ++w) { s += S.st[w][bb * 2]; q += S.st[w][bb * 2 + 1]; }
        const float mean = s * (1.f / D);
        const float var = fmaxf(q * (1.f / D) - mean * mean, 0.f);
        const float mb = rbf(mean), den = rbf(sqrtf(rbf(rbf(var) + 1e-5f)));
        pre[bb] = rbf(((bb ? d1 : d0) - mb * cs) / den);
      }
      const float mine = (c16 & 1) ? pre[1] : pre[0];
      const float up = upper32(mine);
      const float t = 0.7978845608028654f * (mine + 0.044715f * mine * mine * mine);
      const float th = 1.f - 2.f / (__builtin_amdgcn_exp2f(t * 2.8853900817779268f) + 1.f);
      const float gval = rbf(rbf(0.5f * mine * (1.f + th)) * up);
      if (half == 0 && c16 < 2) ((uint16_t*)&S.gp[c16][0])[2 * wave + r4] = (uint16_t)(__float_as_uint(gval) >> 16);
    }
    __syncthreads();
    if (tid < 320) {
      float z00 = 0.f, z01 = 0.f, z10 = 0.f, z11 = 0.f;
#pragma unroll
      for (int k4 = 0; k4 < 3; ++k4) {
        const u32x4 oa = *(const u32x4*)&S.gp[0][k4 * 4], ob = *(const u32x4*)&S.gp[1][k4 * 4];
#pragma unroll
        for (int k = 0; k < 4; ++k) {
          const u32x2 w = *(const u32x2*)&S.wst[48 * GUS + (k4 * 4 + k) * 640 + 2 * tid];
          z00 = dot2(w[0], oa[k], z00); z01 = dot2(w[1], oa[k], z01);
          z10 = dot2(w[0], ob[k], z10); z11 = dot2(w[1], ob[k], z11);
        }
      }
      unsigned char* pz = P.ws + WS_Z + ((size_t)fidx * 640 + gslot) * 16;
      st_gran(pz, z00, z01, ta, tb);
      st_gran(pz + 64, z10, z11, ta, tb);
    }
    STAMP(11);
  }
  if (isR) {
    float y0 = 0.f, y1 = 0.f;
    if (tid < 512) {
      const unsigned char* p0 = P.ws + WS_Z + ((size_t)(tid >> 3) * 640 + rid * 8 + (tid & 7)) * 16;
      u32x4 ga;
      for (int it = 0; it < SPIN_LIMIT; ++it) {
        ld_gran1(p0, ga);
        if (__all(TAGOK(ga))) break;
        __builtin_amdgcn_s_sleep(1);
      }
      STAMP(12);
      y0 = __uint_as_float(ga[0]); y1 = __uint_as_float(ga[2]);
      y0 += dpp<XOR8>(y0); y1 += dpp<XOR8>(y1);
      y0 = x1632sum(y0); y1 = x1632sum(y1);
      if (lane < 8) *(f32x2*)&S.red[wave][lane * 2] = f32x2{y0, y1};
    }
    __syncthreads();
    if (tid < 8) {
      float a0 = 0.f, a1 = 0.f;
#pragma unroll
      for (int w = 0; w < 8; ++w) { a0 += S.red[w][tid * 2]; a1 += S.red[w][tid * 2 + 1]; }
      const u32 o = pk2(S.hres[tid * 2] + rbf(a0), S.hres[tid * 2 + 1] + rbf(a1));
      ((u32*)(P.out + (tid >> 2) * D + rid * 8))[tid & 3] = o;
    }
    if (wg == 0 && tid == 0) {
      u32x2 ne = {ta, tb};
      asm volatile("global_store_dwordx2 %0, %1, off sc1" :: "v"(P.ws + WS_EPOCH), "v"(ne) : "memory");
    }
    STAMP(13);
  }
}
}

extern "C" void kernel_launch(void* const* d_in, const int* in_sizes, int n_in, void* d_out, int out_size,
                              void* d_ws, size_t ws_size, hipStream_t stream) {
  Params P;
  P.x = (const uint16_t*)d_in[0]; P.kc = (const uint16_t*)d_in[1]; P.vc = (const uint16_t*)d_in[2];
  P.wq = (const uint16_t*)d_in[3]; P.wo = (const uint16_t*)d_in[4];
  P.wg = (const uint16_t*)d_in[6]; P.wu = (const uint16_t*)d_in[7]; P.wd = (const uint16_t*)d_in[8];
  P.out = (uint16_t*)d_out; P.ws = (unsigned char*)d_ws;
  fused_kernel<<<NWG, NT, 0, stream>>>(P);
}
```

```cpp
#include <hip/hip_runtime.h>
#include <hip/hip_bf16.h>
#include <stdint.h>
#include <stdio.h>

namespace {
typedef unsigned u32;
typedef u32 u32x2 __attribute__((ext_vector_type(2)));
typedef u32 u32x4 __attribute__((ext_vector_type(4)));
typedef float f32x2 __attribute__((ext_vector_type(2)));
typedef float f32x4 __attribute__((ext_vector_type(4)));
typedef __bf16 bf16x2 __attribute__((ext_vector_type(2)));

constexpr int D = 640, H = 32, SK = 8192, DH = 192, W = 2048, F = 1536, QN = H * DH;
constexpr int NWG = 256, NT = 768, NRED = 80;
#define PACED 0
#define PWIN 3
#define GUAUX 2
constexpr int SPIN_LIMIT = 1 << 20;

constexpr size_t WS_EPOCH = 0;
constexpr size_t WS_Q = 4096;
constexpr size_t WS_O = WS_Q + (size_t)32 * 2 * 480 * 16;
constexpr size_t WS_H = WS_O + (size_t)256 * 128 * 16;
constexpr size_t WS_Y = WS_H + 640 * 16;
constexpr size_t WS_Z = WS_Y + (size_t)192 * 640 * 16;
constexpr size_t WS_END = WS_Z + (size_t)64 * 640 * 16;

__device__ __forceinline__ float rbf(float f) { return __bfloat162float(__float2bfloat16(f)); }
__device__ __forceinline__ float lo2f(u32 v) { return __uint_as_float(v << 16); }
__device__ __forceinline__ float hi2f(u32 v) { return __uint_as_float(v & 0xffff0000u); }
__device__ __forceinline__ u32 pk2(float lo, float hi) {
  u32 r; asm("v_cvt_pk_bf16_f32 %0, %1, %2" : "=v"(r) : "v"(lo), "v"(hi)); return r;
}
__device__ __forceinline__ float dot2(u32 a, u32 b, float c) {
  return __builtin_amdgcn_fdot2_f32_bf16(__builtin_bit_cast(bf16x2, a), __builtin_bit_cast(bf16x2, b), c, false);
}
__device__ __forceinline__ u32 plo(u32 w0, u32 w1) { return __builtin_amdgcn_perm(w1, w0, 0x05040100); }
__device__ __forceinline__ u32 phi(u32 w0, u32 w1) { return __builtin_amdgcn_perm(w1, w0, 0x07060302); }
template <int CTRL> __device__ __forceinline__ float dpp(float x) {
  return __builtin_bit_cast(float, __builtin_amdgcn_mov_dpp(__builtin_bit_cast(int, x), CTRL, 0xf, 0xf, true));
}
constexpr int XOR1 = 0xB1, XOR2 = 0x4E, XOR7 = 0x141, XOR8 = 0x128;
__device__ __forceinline__ float sw16sum(float a, float b) {
  auto s = __builtin_amdgcn_permlane16_swap(__float_as_uint(a), __float_as_uint(b), false, false);
  return __uint_as_float(s[0]) + __uint_as_float(s[1]);
}
__device__ __forceinline__ float sw32sum(float a, float b) {
  auto s = __builtin_amdgcn_permlane32_swap(__float_as_uint(a), __float_as_uint(b), false, false);
  return __uint_as_float(s[0]) + __uint_as_float(s[1]);
}
__device__ __forceinline__ float upper32(float a) {
  auto s = __builtin_amdgcn_permlane32_swap(__float_as_uint(a), __float_as_uint(a), false, false);
  return __uint_as_float(s[1]);
}
__device__ __forceinline__ float x1632max(float x) {
  auto s = __builtin_amdgcn_permlane16_swap(__float_as_uint(x), __float_as_uint(x), false, false);
  x = fmaxf(__uint_as_float(s[0]), __uint_as_float(s[1]));
  auto t = __builtin_amdgcn_permlane32_swap(__float_as_uint(x), __float_as_uint(x), false, false);
  return fmaxf(__uint_as_float(t[0]), __uint_as_float(t[1]));
}
__device__ __forceinline__ float x1632sum(float x) { x = sw16sum(x, x); return sw32sum(x, x); }
__device__ __forceinline__ float row16sum(float x) {
  x += dpp<XOR1>(x); x += dpp<XOR2>(x); x += dpp<XOR7>(x); x += dpp<XOR8>(x); return x;
}
__device__ __forceinline__ float wave_sum(float x) { return x1632sum(row16sum(x)); }
__device__ __forceinline__ float wave_max(float x) {
  x = fmaxf(x, dpp<XOR1>(x)); x = fmaxf(x, dpp<XOR2>(x)); x = fmaxf(x, dpp<XOR7>(x)); x = fmaxf(x, dpp<XOR8>(x));
  return x1632max(x);
}
__device__ __forceinline__ void st_gran(unsigned char* p, float v0, float v1, u32 ta, u32 tb) {
  u32x4 g = {__float_as_uint(v0), ta, __float_as_uint(v1), tb};
  asm volatile("global_store_dwordx4 %0, %1, off sc1\n\ts_nop 1" :: "v"(p), "v"(g) : "memory");
}
__device__ __forceinline__ void ld_gran1(const unsigned char* p, u32x4& a) {
  asm volatile("global_load_dwordx4 %0, %1, off sc1\n\ts_waitcnt vmcnt(0)" : "=&v"(a) : "v"(p) : "memory");
}
__device__ __forceinline__ void ld_gran2(const unsigned char* p, const unsigned char* q, u32x4& a, u32x4& b) {
  asm volatile("global_load_dwordx4 %0, %2, off sc1\n\tglobal_load_dwordx4 %1, %3, off sc1\n\ts_waitcnt vmcnt(0)"
               : "=&v"(a), "=&v"(b) : "v"(p), "v"(q) : "memory");
}
__device__ __forceinline__ void ld_gran3(const unsigned char* p0, const unsigned char* p1, const unsigned char* p2,
                                         u32x4& a, u32x4& b, u32x4& c) {
  asm volatile("global_load_dwordx4 %0, %3, off sc1\n\tglobal_load_dwordx4 %1, %4, off sc1\n\t"
               "global_load_dwordx4 %2, %5, off sc1\n\ts_waitcnt vmcnt(0)"
               : "=&v"(a), "=&v"(b), "=&v"(c) : "v"(p0), "v"(p1), "v"(p2) : "memory");
}
__device__ __forceinline__ void ld_gran10(const unsigned char* p0, const unsigned char* p0b, const unsigned char* p1,
                                          const unsigned char* p1b, u32x4 (&g)[10]) {
  asm volatile(
      "global_load_dwordx4 %0, %10, off sc1\n\tglobal_load_dwordx4 %1, %10, off offset:1536 sc1\n\t"
      "global_load_dwordx4 %2, %10, off offset:3072 sc1\n\tglobal_load_dwordx4 %3, %11, off sc1\n\t"
      "global_load_dwordx4 %4, %11, off offset:1536 sc1\n\t"
      "global_load_dwordx4 %5, %12, off sc1\n\tglobal_load_dwordx4 %6, %12, off offset:1536 sc1\n\t"
      "global_load_dwordx4 %7, %12, off offset:3072 sc1\n\tglobal_load_dwordx4 %8, %13, off sc1\n\t"
      "global_load_dwordx4 %9, %13, off offset:1536 sc1\n\ts_waitcnt vmcnt(0)"
      : "=&v"(g[0]), "=&v"(g[1]), "=&v"(g[2]), "=&v"(g[3]), "=&v"(g[4]), "=&v"(g[5]), "=&v"(g[6]), "=&v"(g[7]),
        "=&v"(g[8]), "=&v"(g[9])
      : "v"(p0), "v"(p0b), "v"(p1), "v"(p1b) : "memory");
}
__device__ __forceinline__ __amdgpu_buffer_rsrc_t mkrsrc(const void* p, u32 bytes) {
  return __builtin_amdgcn_make_buffer_rsrc(const_cast<void*>(p), 0, bytes, 0x00020000);
}
#define TAGOK(g) ((g)[1] == ta && (g)[3] == tb)
#define LDS_LD(p) __hip_atomic_load((p), __ATOMIC_RELAXED, __HIP_MEMORY_SCOPE_WORKGROUP)

#define DIAG 1
#define STAMPT(t, i) do { if (tid == (t)) { __hip_atomic_store(&((unsigned long long*)(P.ws + (64u << 20)))[wg * 24 + (i)], (unsigned long long)wall_clock64(), __ATOMIC_RELAXED, __HIP_MEMORY_SCOPE_AGENT); } } while (0)
#define STAMP(i) STAMPT(0, i)
struct Params {
  const uint16_t* x; const uint16_t* kc; const uint16_t* vc; const uint16_t* wq; const uint16_t* wo;
  const uint16_t* wg; const uint16_t* wu; const uint16_t* wd; uint16_t* out; unsigned char* ws;
};

constexpr int GUS = 324;
struct Lds {
  u32 wst[48 * GUS + 12 * 640];
  u32 xnp[2][320];
  float qpart[8][2][192];
  u32 qpk[96];
  float pl[9][64];
  float ml[10][2];
  float red[18][192];
  u32 opk[2][16];
  float wsum[8][16];
  float st[5][4];
  float hres[16];
  u32 hp[2][320];
  u32 gp[2][12];
  u32 qcnt, qflag, ocnt, pad1;
};

__global__ void __launch_bounds__(NT, 3) fused_kernel(Params P) {
  __shared__ __attribute__((aligned(16))) Lds S;
  const int tid = threadIdx.x, lane = tid & 63, wave = __builtin_amdgcn_readfirstlane(tid >> 6);
  const int half = lane >> 5, c = lane & 31;
  const int r4 = lane >> 4, c16 = lane & 15;
  const int wg = blockIdx.x, xcd = wg & 7, idx = wg >> 3;
  const int h = (idx >> 3) * 8 + xcd, j8 = idx & 7, b = j8 >> 2, sp = j8 & 3;
  const int prod = h * 8 + j8;
  const bool isF = sp == 3, isO = !isF;
  const int jr = j8 - b;
  const bool isQ = isO && jr < 5;
  const int fidx = h * 2 + b;
  const int rid = h * 5 + jr;
  const bool isR = isQ && rid < NRED;

  STAMP(0);
  if (tid == 0) { S.qcnt = 0u; S.qflag = 0u; S.ocnt = 0u; }
  __syncthreads();
  const unsigned long long ep = __hip_atomic_load((const unsigned long long*)(P.ws + WS_EPOCH), __ATOMIC_RELAXED, __HIP_MEMORY_SCOPE_AGENT);
  const u32 ta = (u32)ep + 1u, tb = (u32)(ep >> 32) + 1u;
  const u32 xres = ((const u32*)(P.x + ((tid >> 2) & 1) * D + (rid % NRED) * 8))[tid & 3];
  auto stream_body = [&](const int slotw, const int ng, const int row0, const bool waitq) {
    const size_t kvoff = ((size_t)(b * H + h) * SK + (SK - W) + sp * 544 + row0) * DH;
    const __amdgpu_buffer_rsrc_t krs = mkrsrc(P.kc + kvoff, ng * 1536);
    const __amdgpu_buffer_rsrc_t vrs = mkrsrc(P.vc + kvoff, ng * 1536);
    const u32 loA = r4 * 384 + c16 * 16, loB = r4 * 384 + 256 + c16 * 8;
    u32x4 ka[16]; u32x2 kb[16];
#pragma unroll
    for (int i = 0; i < 16; ++i) {
      ka[i] = __builtin_amdgcn_raw_buffer_load_b128(krs, loA, i * 1536, 2);
      kb[i] = __builtin_amdgcn_raw_buffer_load_b64(krs, loB, i * 1536, 2);
    }
    u32x4 va[16]; u32x2 vb[16];
#pragma unroll
    for (int i = 0; i < 8; ++i) {
      va[i] = __builtin_amdgcn_raw_buffer_load_b128(vrs, loA, i * 1536, 2);
      vb[i] = __builtin_amdgcn_raw_buffer_load_b64(vrs, loB, i * 1536, 2);
    }
    __builtin_amdgcn_sched_barrier(0);
    STAMP(1);
    if (waitq) { while (LDS_LD(&S.qflag) == 0u) __builtin_amdgcn_s_sleep(1); }
    asm volatile("" ::: "memory");
    STAMP(2);
    const u32x4 qa = *(const u32x4*)&S.qpk[4 * c16];
    const u32x2 qb = *(const u32x2*)&S.qpk[64 + 2 * c16];
    float d[16];
#pragma unroll
    for (int i = 0; i < 16; ++i) {
      float t = dot2(ka[i][0], qa[0], 0.f);
      t = dot2(ka[i][1], qa[1], t); t = dot2(ka[i][2], qa[2], t); t = dot2(ka[i][3], qa[3], t);
      t = dot2(kb[i][0], qb[0], t); d[i] = dot2(kb[i][1], qb[1], t);
    }
    __builtin_amdgcn_sched_barrier(0);
#pragma unroll
    for (int i = 8; i < 16; ++i) {
      va[i] = __builtin_amdgcn_raw_buffer_load_b128(vrs, loA, i * 1536, 2);
      vb[i] = __builtin_amdgcn_raw_buffer_load_b64(vrs, loB, i * 1536, 2);
    }
    __builtin_amdgcn_sched_barrier(0);
#define TSTEP(A, N, CTRL, BIT)                                                                 \
  _Pragma("unroll") for (int i = 0; i < N; ++i) {                                              \
    const bool hb = (c & BIT) != 0;                                                            \
    const float keep = hb ? A[i + N] : A[i], send = hb ? A[i] : A[i + N];                      \
    A[i] = keep + dpp<CTRL>(send);                                                             \
  }
    TSTEP(d, 8, XOR8, 8) TSTEP(d, 4, XOR7, 4) TSTEP(d, 2, XOR2, 2) TSTEP(d, 1, XOR1, 1)
    const float sc = c16 < ng ? rbf(rbf(d[0]) * 0.07216878364870322f) * 1.4426950408889634f : -INFINITY;
    const float mw = wave_max(sc);
    const float p = __builtin_amdgcn_exp2f(sc - mw);
    const float lw = wave_sum(p);
    S.pl[slotw][lane] = p;
    if (lane == 0) { S.ml[slotw][0] = mw; S.ml[slotw][1] = lw; }
    STAMP(3);
    asm volatile("s_waitcnt lgkmcnt(0)" ::: "memory");
    float acc[12];
#pragma unroll
    for (int k = 0; k < 12; ++k) acc[k] = 0.f;
#pragma unroll
    for (int i4 = 0; i4 < 4; ++i4) {
      const f32x4 pp = *(const f32x4*)&S.pl[slotw][r4 * 16 + i4 * 4];
#pragma unroll
      for (int j = 0; j < 4; ++j) {
        const u32x4 v = va[i4 * 4 + j]; const u32x2 w = vb[i4 * 4 + j];
#pragma unroll
        for (int k = 0; k < 4; ++k) {
          acc[2 * k] = fmaf(pp[j], lo2f(v[k]), acc[2 * k]);
          acc[2 * k + 1] = fmaf(pp[j], hi2f(v[k]), acc[2 * k + 1]);
        }
#pragma unroll
        for (int k = 0; k < 2; ++k) {
          acc[8 + 2 * k] = fmaf(pp[j], lo2f(w[k]), acc[8 + 2 * k]);
          acc[8 + 2 * k + 1] = fmaf(pp[j], hi2f(w[k]), acc[8 + 2 * k + 1]);
        }
      }
    }
#pragma unroll
    for (int k = 0; k < 6; ++k) acc[k] = sw32sum(acc[k], acc[k + 6]);
    float* dst = &S.red[slotw * 2 + (r4 & 1)][0];
    if (half == 0) {
      *(f32x4*)&dst[8 * c16] = f32x4{acc[0], acc[1], acc[2], acc[3]};
      *(f32x2*)&dst[8 * c16 + 4] = f32x2{acc[4], acc[5]};
    } else {
      *(f32x2*)&dst[8 * c16 + 6] = f32x2{acc[0], acc[1]};
      *(f32x4*)&dst[128 + 4 * c16] = f32x4{acc[2], acc[3], acc[4], acc[5]};
    }
    STAMP(4); STAMPT(448, 19); STAMPT(256, 20); STAMPT(704, 18);
  };

  if (wave == 11 && isO) {
    stream_body(8, 8, 512, true);
  } else if (wave >= 8) {
    __builtin_amdgcn_s_setprio(3);
    const int st = tid - 512;
    if (isQ) {
      u32 xr[2][5];
#pragma unroll
      for (int bb = 0; bb < 2; ++bb) {
        const u32* xp = (const u32*)(P.x + bb * D) + lane * 5;
#pragma unroll
        for (int i = 0; i < 5; ++i) xr[bb][i] = xp[i];
      }
      const int s2 = st < 192 ? st : 191, cc = s2 % 24, slot = s2 / 24;
      u32x4 wqr[16];
      {
        const __amdgpu_buffer_rsrc_t rs = mkrsrc(P.wq + (size_t)(jr * 128) * QN + h * DH, 128 * QN * 2);
        const u32 vo = (u32)(slot * 16) * (QN * 2) + cc * 16;
#pragma unroll
        for (int i = 0; i < 16; ++i) wqr[i] = __builtin_amdgcn_raw_buffer_load_b128(rs, vo, i * QN * 2, 2);
      }
#pragma unroll
      for (int bb = 0; bb < 2; ++bb) {
        float xv[10];
#pragma unroll
        for (int i = 0; i < 5; ++i) { xv[2 * i] = lo2f(xr[bb][i]); xv[2 * i + 1] = hi2f(xr[bb][i]); }
        float s = 0.f;
#pragma unroll
        for (int i = 0; i < 10; ++i) s += xv[i];
        const float mean = wave_sum(s) * (1.f / D);
        float q = 0.f;
#pragma unroll
        for (int i = 0; i < 10; ++i) { const float d = xv[i] - mean; q += d * d; }
        const float var = wave_sum(q) * (1.f / D);
        const float mb = rbf(mean), den = rbf(sqrtf(rbf(rbf(var) + 1e-5f)));
#pragma unroll
        for (int i = 0; i < 5; ++i) S.xnp[bb][lane * 5 + i] = pk2(rbf(xv[2 * i] - mb) / den, rbf(xv[2 * i + 1] - mb) / den);
      }
      asm volatile("s_waitcnt lgkmcnt(0)" ::: "memory");
      float a[2][8];
#pragma unroll
      for (int k = 0; k < 8; ++k) { a[0][k] = 0.f; a[1][k] = 0.f; }
#pragma unroll
      for (int i4 = 0; i4 < 2; ++i4) {
        const u32x4 x0 = *(const u32x4*)&S.xnp[0][jr * 64 + slot * 8 + i4 * 4];
        const u32x4 x1 = *(const u32x4*)&S.xnp[1][jr * 64 + slot * 8 + i4 * 4];
#pragma unroll
        for (int i = 0; i < 4; ++i) {
          const u32x4 w0 = wqr[(i4 * 4 + i) * 2], w1 = wqr[(i4 * 4 + i) * 2 + 1];
#pragma unroll
          for (int k = 0; k < 4; ++k) {
            const u32 wl = plo(w0[k], w1[k]), wh = phi(w0[k], w1[k]);
            a[0][2 * k] = dot2(wl, x0[i], a[0][2 * k]); a[0][2 * k + 1] = dot2(wh, x0[i], a[0][2 * k + 1]);
            a[1][2 * k] = dot2(wl, x1[i], a[1][2 * k]); a[1][2 * k + 1] = dot2(wh, x1[i], a[1][2 * k + 1]);
          }
        }
      }
      if (st < 192) {
#pragma unroll
        for (int bb = 0; bb < 2; ++bb) {
          *(f32x4*)&S.qpart[slot][bb][cc * 8] = f32x4{a[bb][0], a[bb][1], a[bb][2], a[bb][3]};
          *(f32x4*)&S.qpart[slot][bb][cc * 8 + 4] = f32x4{a[bb][4], a[bb][5], a[bb][6], a[bb][7]};
        }
      }
      asm volatile("s_waitcnt lgkmcnt(0)" ::: "memory");
      if (lane == 0) __hip_atomic_fetch_add(&S.qcnt, 1u, __ATOMIC_RELAXED, __HIP_MEMORY_SCOPE_WORKGROUP);
      while (LDS_LD(&S.qcnt) != 3u) __builtin_amdgcn_s_sleep(1);
      asm volatile("" ::: "memory");
      STAMPT(512, 14);
      if (st < 192) {
        const int bb = st / 96, k = st % 96;
        float s0 = 0.f, s1 = 0.f;
#pragma unroll
        for (int w = 0; w < 8; ++w) { const f32x2 t = *(const f32x2*)&S.qpart[w][bb][2 * k]; s0 += t[0]; s1 += t[1]; }
        st_gran(P.ws + WS_Q + ((size_t)((h * 2 + bb) * 5 + jr) * 96 + k) * 16, s0, s1, ta, tb);
      }
    }
    if (wave == 8) {
      const unsigned char* pq = P.ws + WS_Q + (size_t)(h * 2 + b) * 480 * 16;
      const unsigned char* p0 = pq + lane * 16;
      const unsigned char* p1 = pq + (64 + c) * 16;
      u32x4 g[10];
      for (int it = 0; it < SPIN_LIMIT; ++it) {
        ld_gran10(p0, p0 + 4608, p1, p1 + 4608, g);
        bool ok = true;
#pragma unroll
        for (int i = 0; i < 10; ++i) ok = ok && TAGOK(g[i]);
        if (__all(ok)) break;
        __builtin_amdgcn_s_sleep(2);
      }
      STAMPT(512, 15);
      float s0 = 0.f, s1 = 0.f, t0 = 0.f, t1 = 0.f;
#pragma unroll
      for (int i = 0; i < 5; ++i) {
        s0 += __uint_as_float(g[i][0]); s1 += __uint_as_float(g[i][2]);
        t0 += __uint_as_float(g[5 + i][0]); t1 += __uint_as_float(g[5 + i][2]);
      }
      S.qpk[lane] = pk2(s0, s1);
      if (lane < 32) S.qpk[64 + lane] = pk2(t0, t1);
      asm volatile("s_waitcnt lgkmcnt(0)" ::: "memory");
      if (lane == 0) __hip_atomic_store(&S.qflag, 1u, __ATOMIC_RELAXED, __HIP_MEMORY_SCOPE_WORKGROUP);
    } else {
      __builtin_amdgcn_s_setprio(3);
      const int tw = st - 64;
      if (isO) {
        const __amdgpu_buffer_rsrc_t ro = mkrsrc(P.wo + (size_t)(h * DH + jr * 32) * D, 32 * D * 2);
        u32x4 w0[10], w1[10];
#pragma unroll
        for (int i = 0; i < 10; ++i) {
          const int task = tw + 128 * i, k = task / 80, ch = task % 80;
          w0[i] = __builtin_amdgcn_raw_buffer_load_b128(ro, (u32)(2 * k) * (D * 2) + ch * 16, 0, 2);
          w1[i] = __builtin_amdgcn_raw_buffer_load_b128(ro, (u32)(2 * k) * (D * 2) + ch * 16, D * 2, 2);
        }
#pragma unroll
        for (int i = 0; i < 10; ++i) {
          const int task = tw + 128 * i, k = task / 80, ch = task % 80;
          u32* d = &S.wst[k * 640 + ch * 8];
          *(u32x4*)d = u32x4{plo(w0[i][0], w1[i][0]), phi(w0[i][0], w1[i][0]), plo(w0[i][1], w1[i][1]), phi(w0[i][1], w1[i][1])};
          *(u32x4*)(d + 4) = u32x4{plo(w0[i][2], w1[i][2]), phi(w0[i][2], w1[i][2]), plo(w0[i][3], w1[i][3]), phi(w0[i][3], w1[i][3])};
        }
      } else {
        {
          const __amdgpu_buffer_rsrc_t rg = mkrsrc(P.wg + fidx * 24, (D * F - fidx * 24) * 2);
          u32x4 a0[5], a1[5];
#pragma unroll
          for (int i = 0; i < 5; ++i) {
            const int task = tw + 192 * i, k = task / 3, q = task % 3;
            const u32 vo = (u32)(2 * k) * (F * 2) + q * 16;
            a0[i] = __builtin_amdgcn_raw_buffer_load_b128(rg, vo, 0, GUAUX);
            a1[i] = __builtin_amdgcn_raw_buffer_load_b128(rg, vo, F * 2, GUAUX);
          }
#pragma unroll
          for (int i = 0; i < 5; ++i) {
            const int task = tw + 192 * i, k = task / 3, q = task % 3;
#pragma unroll
            for (int e = 0; e < 4; ++e) {
              S.wst[(q * 8 + 2 * e) * GUS + k] = plo(a0[i][e], a1[i][e]);
              S.wst[(q * 8 + 2 * e + 1) * GUS + k] = phi(a0[i][e], a1[i][e]);
            }
          }
        }
        const __amdgpu_buffer_rsrc_t rd = mkrsrc(P.wd + (size_t)(fidx * 24) * D, 24 * D * 2);
        u32x4 w0[5], w1[5];
#pragma unroll
        for (int i = 0; i < 5; ++i) {
          const int task = tw + 192 * i, k = task / 80, ch = task % 80;
          w0[i] = __builtin_amdgcn_raw_buffer_load_b128(rd, (u32)(2 * k) * (D * 2) + ch * 16, 0, 2);
          w1[i] = __builtin_amdgcn_raw_buffer_load_b128(rd, (u32)(2 * k) * (D * 2) + ch * 16, D * 2, 2);
        }
#pragma unroll
        for (int i = 0; i < 5; ++i) {
          const int task = tw + 192 * i, k = task / 80, ch = task % 80;
          u32* d = &S.wst[48 * GUS + k * 640 + ch * 8];
          *(u32x4*)d = u32x4{plo(w0[i][0], w1[i][0]), phi(w0[i][0], w1[i][0]), plo(w0[i][1], w1[i][1]), phi(w0[i][1], w1[i][1])};
          *(u32x4*)(d + 4) = u32x4{plo(w0[i][2], w1[i][2]), phi(w0[i][2], w1[i][2]), plo(w0[i][3], w1[i][3]), phi(w0[i][3], w1[i][3])};
        }
      }
      STAMPT(576, 16);
    }
  } else {
    stream_body(wave, isF ? 13 : 16, isF ? wave * 52 : wave * 64, true);
  }
  __syncthreads();
  if (tid < 128) {
    float mm[9];
    float Mwg = -INFINITY;
#pragma unroll
    for (int w = 0; w < 9; ++w) { mm[w] = (w < 8 || isO) ? S.ml[w][0] : -INFINITY; Mwg = fmaxf(Mwg, mm[w]); }
    float Lwg = 0.f;
#pragma unroll
    for (int w = 0; w < 9; ++w) { mm[w] = __builtin_amdgcn_exp2f(mm[w] - Mwg); Lwg = fmaf(mm[w], (w < 8 || isO) ? S.ml[w][1] : 0.f, Lwg); }
    const int t = tid < 97 ? (tid > 0 ? tid - 1 : 0) : 95;
    float o0 = 0.f, o1 = 0.f;
#pragma unroll
    for (int w = 0; w < 9; ++w) {
      if (w < 8 || isO) {
        const f32x2 a = *(const f32x2*)&S.red[2 * w][2 * t], bb = *(const f32x2*)&S.red[2 * w + 1][2 * t];
        o0 = fmaf(mm[w], a[0] + bb[0], o0); o1 = fmaf(mm[w], a[1] + bb[1], o1);
      }
    }
    if (tid == 0) { o0 = Mwg; o1 = Lwg; }
    if (tid < 97) st_gran(P.ws + WS_O + ((size_t)prod * 128 + tid) * 16, o0, o1, ta, tb);
    if (lane == 0) __hip_atomic_fetch_add(&S.ocnt, 1u, __ATOMIC_RELAXED, __HIP_MEMORY_SCOPE_WORKGROUP);
    STAMP(5);
    if (isO) {
      const unsigned char* pb = P.ws + WS_O + (size_t)(h * 8 + wave * 4 + r4) * 128 * 16;
      u32x4 go, gm;
      for (int it = 0; it < SPIN_LIMIT; ++it) {
        ld_gran2(pb + (1 + jr * 16 + c16) * 16, pb, go, gm);
        if (__all(TAGOK(go) && TAGOK(gm))) break;
        __builtin_amdgcn_s_sleep(1);
      }
      const float m = __uint_as_float(gm[0]), l = __uint_as_float(gm[2]);
      const float M = x1632max(m);
      const float e = __builtin_amdgcn_exp2f(m - M);
      const float L = x1632sum(e * l);
      const float a0 = x1632sum(e * __uint_as_float(go[0])), a1 = x1632sum(e * __uint_as_float(go[2]));
      if (lane < 16) S.opk[wave][lane] = pk2(a0 / L, a1 / L);
    }
  }
  const int gslot = (tid >> 2) * 8 + (tid & 3);
  if (isO) {
    __syncthreads();
    STAMP(6);
    if (tid < 320) {
      float y00 = 0.f, y01 = 0.f, y10 = 0.f, y11 = 0.f;
#pragma unroll
      for (int k4 = 0; k4 < 4; ++k4) {
        const u32x4 oa = *(const u32x4*)&S.opk[0][k4 * 4], ob = *(const u32x4*)&S.opk[1][k4 * 4];
#pragma unroll
        for (int k = 0; k < 4; ++k) {
          const u32x2 w = *(const u32x2*)&S.wst[(k4 * 4 + k) * 640 + 2 * tid];
          y00 = dot2(w[0], oa[k], y00); y01 = dot2(w[1], oa[k], y01);
          y10 = dot2(w[0], ob[k], y10); y11 = dot2(w[1], ob[k], y11);
        }
      }
      unsigned char* py = P.ws + WS_Y + ((size_t)(h * 6 + jr) * 640 + gslot) * 16;
      st_gran(py, y00, y01, ta, tb);
      st_gran(py + 64, y10, y11, ta, tb);
    }
    STAMP(7);
  }
  if (isR) {
    if (tid < 512) {
      const unsigned char* p0 = P.ws + WS_Y + ((size_t)(tid >> 3) * 640 + rid * 8 + (tid & 7)) * 16;
      constexpr size_t ST = (size_t)64 * 640 * 16;
      u32x4 ga, gb, gc;
      for (int it = 0; it < SPIN_LIMIT; ++it) {
        ld_gran3(p0, p0 + ST, p0 + 2 * ST, ga, gb, gc);
        if (__all(TAGOK(ga) && TAGOK(gb) && TAGOK(gc))) break;
        __builtin_amdgcn_s_sleep(1);
      }
      STAMP(8);
      float y0 = __uint_as_float(ga[0]) + __uint_as_float(gb[0]) + __uint_as_float(gc[0]);
      float y1 = __uint_as_float(ga[2]) + __uint_as_float(gb[2]) + __uint_as_float(gc[2]);
      y0 += dpp<XOR8>(y0); y1 += dpp<XOR8>(y1);
      y0 = x1632sum(y0); y1 = x1632sum(y1);
      if (lane < 8) *(f32x2*)&S.wsum[wave][lane * 2] = f32x2{y0, y1};
    }
    __syncthreads();
    if (tid < 8) {
      float a0 = 0.f, a1 = 0.f;
#pragma unroll
      for (int w = 0; w < 8; ++w) { a0 += S.wsum[w][tid * 2]; a1 += S.wsum[w][tid * 2 + 1]; }
      const float h0 = rbf(lo2f(xres) + rbf(a0)), h1 = rbf(hi2f(xres) + rbf(a1));
      S.hres[tid * 2] = h0; S.hres[tid * 2 + 1] = h1;
      st_gran(P.ws + WS_H + (size_t)(rid * 8 + tid) * 16, h0, h1, ta, tb);
    }
    STAMP(9);
  }
  if (isF) {
    if (wave >= 8) {
      while (LDS_LD(&S.ocnt) != 2u) __builtin_amdgcn_s_sleep(1);
      asm volatile("" ::: "memory");
      const int st = tid - 512;
      const __amdgpu_buffer_rsrc_t ru = mkrsrc(P.wu + fidx * 24, (D * F - fidx * 24) * 2);
      u32x4 a0[4], a1[4];
#pragma unroll
      for (int i = 0; i < 4; ++i) {
        const int task = st + 256 * i, k = task / 3, q = task % 3;
        const u32 vo = task < 960 ? (u32)(2 * k) * (F * 2) + q * 16 : 0x7fffff00u;
        a0[i] = __builtin_amdgcn_raw_buffer_load_b128(ru, vo, 0, GUAUX);
        a1[i] = __builtin_amdgcn_raw_buffer_load_b128(ru, vo, F * 2, GUAUX);
      }
#pragma unroll
      for (int i = 0; i < 4; ++i) {
        const int task = st + 256 * i, k = task / 3, q = task % 3;
        if (task < 960) {
#pragma unroll
          for (int e = 0; e < 4; ++e) {
            S.wst[(24 + q * 8 + 2 * e) * GUS + k] = plo(a0[i][e], a1[i][e]);
            S.wst[(24 + q * 8 + 2 * e + 1) * GUS + k] = phi(a0[i][e], a1[i][e]);
          }
        }
      }
      STAMPT(512, 17);
    }
    if (tid < 320) {
      const unsigned char* p0 = P.ws + WS_H + (size_t)gslot * 16;
      u32x4 ga, gb;
      for (int it = 0; it < SPIN_LIMIT; ++it) {
        ld_gran2(p0, p0 + 64, ga, gb);
        if (__all(TAGOK(ga) && TAGOK(gb))) break;
        __builtin_amdgcn_s_sleep(1);
      }
      STAMP(10);
      const float h00 = __uint_as_float(ga[0]), h01 = __uint_as_float(ga[2]), h10 = __uint_as_float(gb[0]), h11 = __uint_as_float(gb[2]);
      S.hp[0][tid] = (ga[0] >> 16) | (ga[2] & 0xffff0000u);
      S.hp[1][tid] = (gb[0] >> 16) | (gb[2] & 0xffff0000u);
      float s0 = h00 + h01, q0 = h00 * h00 + h01 * h01, s1 = h10 + h11, q1 = h10 * h10 + h11 * h11;
      s0 = sw32sum(s0, s1); q0 = sw32sum(q0, q1);
      s0 = sw16sum(s0, q0);
      s0 = row16sum(s0);
      if (c16 == 0) S.st[wave][r4] = s0;
    }
    __syncthreads();
    {
      const u32* wrow = &S.wst[((r4 >> 1) * 24 + 2 * wave + (r4 & 1)) * GUS + 20 * c16];
      float d0 = 0.f, d1 = 0.f, cs = 0.f;
#pragma unroll
      for (int i = 0; i < 5; ++i) {
        const u32x4 w = *(const u32x4*)&wrow[4 * i];
        const u32x4 ha = *(const u32x4*)&S.hp[0][20 * c16 + 4 * i], hb = *(const u32x4*)&S.hp[1][20 * c16 + 4 * i];
#pragma unroll
        for (int k = 0; k < 4; ++k) { d0 = dot2(w[k], ha[k], d0); d1 = dot2(w[k], hb[k], d1); cs = dot2(w[k], 0x3f803f80u, cs); }
      }
      d0 = row16sum(d0); d1 = row16sum(d1); cs = row16sum(cs);
      float pre[2];
#pragma unroll
      for (int bb = 0; bb < 2; ++bb) {
        float s = 0.f, q = 0.f;
#pragma unroll
        for (int w = 0; w < 5; ++w) { s += S.st[w][bb * 2]; q += S.st[w][bb * 2 + 1]; }
        const float mean = s * (1.f / D);
        const float var = fmaxf(q * (1.f / D) - mean * mean, 0.f);
        const float mb = rbf(mean), den = rbf(sqrtf(rbf(rbf(var) + 1e-5f)));
        pre[bb] = rbf(((bb ? d1 : d0) - mb * cs) / den);
      }
      const float mine = (c16 & 1) ? pre[1] : pre[0];
      const float up = upper32(mine);
      const float t = 0.7978845608028654f * (mine + 0.044715f * mine * mine * mine);
      const float th = 1.f - 2.f / (__builtin_amdgcn_exp2f(t * 2.8853900817779268f) + 1.f);
      const float gval = rbf(rbf(0.5f * mine * (1.f + th)) * up);
      if (half == 0 && c16 < 2) ((uint16_t*)&S.gp[c16][0])[2 * wave + r4] = (uint16_t)(__float_as_uint(gval) >> 16);
    }
    __syncthreads();
    if (tid < 320) {
      float z00 = 0.f, z01 = 0.f, z10 = 0.f, z11 = 0.f;
#pragma unroll
      for (int k4 = 0; k4 < 3; ++k4) {
        const u32x4 oa = *(const u32x4*)&S.gp[0][k4 * 4], ob = *(const u32x4*)&S.gp[1][k4 * 4];
#pragma unroll
        for (int k = 0; k < 4; ++k) {
          const u32x2 w = *(const u32x2*)&S.wst[48 * GUS + (k4 * 4 + k) * 640 + 2 * tid];
          z00 = dot2(w[0], oa[k], z00); z01 = dot2(w[1], oa[k], z01);
          z10 = dot2(w[0], ob[k], z10); z11 = dot2(w[1], ob[k], z11);
        }
      }
      unsigned char* pz = P.ws + WS_Z + ((size_t)fidx * 640 + gslot) * 16;
      st_gran(pz, z00, z01, ta, tb);
      st_gran(pz + 64, z10, z11, ta, tb);
    }
    STAMP(11);
  }
  if (isR) {
    float y0 = 0.f, y1 = 0.f;
    if (tid < 512) {
      const unsigned char* p0 = P.ws + WS_Z + ((size_t)(tid >> 3) * 640 + rid * 8 + (tid & 7)) * 16;
      u32x4 ga;
      for (int it = 0; it < SPIN_LIMIT; ++it) {
        ld_gran1(p0, ga);
        if (__all(TAGOK(ga))) break;
        __builtin_amdgcn_s_sleep(1);
      }
      STAMP(12);
      y0 = __uint_as_float(ga[0]); y1 = __uint_as_float(ga[2]);
      y0 += dpp<XOR8>(y0); y1 += dpp<XOR8>(y1);
      y0 = x1632sum(y0); y1 = x1632sum(y1);
      if (lane < 8) *(f32x2*)&S.red[wave][lane * 2] = f32x2{y0, y1};
    }
    __syncthreads();
    if (tid < 8) {
      float a0 = 0.f, a1 = 0.f;
#pragma unroll
      for (int w = 0; w < 8; ++w) { a0 += S.red[w][tid * 2]; a1 += S.red[w][tid * 2 + 1]; }
      const u32 o = pk2(S.hres[tid * 2] + rbf(a0), S.hres[tid * 2 + 1] + rbf(a1));
      ((u32*)(P.out + (tid >> 2) * D + rid * 8))[tid & 3] = o;
    }
    if (wg == 0 && tid == 0) {
      u32x2 ne = {ta, tb};
      asm volatile("global_store_dwordx2 %0, %1, off sc1" :: "v"(P.ws + WS_EPOCH), "v"(ne) : "memory");
    }
    STAMP(13);
    if (wg == 0 && tid == 0) {
      for (int i = 0; i < 200; ++i) __builtin_amdgcn_s_sleep(127);
      unsigned long long* sp = (unsigned long long*)(P.ws + (64u << 20));
      unsigned long long t0 = ~0ull;
      for (int w = 0; w < 256; ++w) { unsigned long long v = __hip_atomic_load(&sp[w * 24], __ATOMIC_RELAXED, __HIP_MEMORY_SCOPE_AGENT); t0 = v < t0 ? v : t0; }
      for (int i = 0; i < 21; ++i) {
        unsigned long long mn = ~0ull, mx = 0;
        for (int w = 0; w < 256; ++w) {
          unsigned long long v = __hip_atomic_load(&sp[w * 24 + i], __ATOMIC_RELAXED, __HIP_MEMORY_SCOPE_AGENT) - t0;
          if (v > 100000ull) continue;
          mn = v < mn ? v : mn; mx = v > mx ? v : mx;
        }
        printf("stamp %d min %d max %d (x10ns)\n", i, (int)mn, (int)mx);
      }
    }
  }
}
}

extern "C" void kernel_launch(void* const* d_in, const int* in_sizes, int n_in, void* d_out, int out_size,
                              void* d_ws, size_t ws_size, hipStream_t stream) {
  Params P;
  P.x = (const uint16_t*)d_in[0]; P.kc = (const uint16_t*)d_in[1]; P.vc = (const uint16_t*)d_in[2];
  P.wq = (const uint16_t*)d_in[3]; P.wo = (const uint16_t*)d_in[4];
  P.wg = (const uint16_t*)d_in[6]; P.wu = (const uint16_t*)d_in[7]; P.wd = (const uint16_t*)d_in[8];
  P.out = (uint16_t*)d_out; P.ws = (unsigned char*)d_ws;
  fused_kernel<<<NWG, NT, 0, stream>>>(P);
}
```

```cpp
#include <hip/hip_runtime.h>
#include <hip/hip_bf16.h>
#include <stdint.h>

namespace {
typedef unsigned u32;
typedef u32 u32x2 __attribute__((ext_vector_type(2)));
typedef u32 u32x4 __attribute__((ext_vector_type(4)));
typedef float f32x2 __attribute__((ext_vector_type(2)));
typedef float f32x4 __attribute__((ext_vector_type(4)));
typedef __bf16 bf16x2 __attribute__((ext_vector_type(2)));

constexpr int D = 640, H = 32, SK = 8192, DH = 192, W = 2048, F = 1536, QN = H * DH;
constexpr int NWG = 256, NT = 768, NRED = 80;
#define PACED 0
#define PWIN 3
#define GUAUX 2
constexpr int SPIN_LIMIT = 1 << 20;

constexpr size_t WS_EPOCH = 0;
constexpr size_t WS_Q = 4096;
constexpr size_t WS_O = WS_Q + (size_t)32 * 2 * 480 * 16;
constexpr size_t WS_H = WS_O + (size_t)256 * 128 * 16;
constexpr size_t WS_Y = WS_H + 640 * 16;
constexpr size_t WS_Z = WS_Y + (size_t)192 * 640 * 16;
constexpr size_t WS_END = WS_Z + (size_t)64 * 640 * 16;

__device__ __forceinline__ float rbf(float f) { return __bfloat162float(__float2bfloat16(f)); }
__device__ __forceinline__ float lo2f(u32 v) { return __uint_as_float(v << 16); }
__device__ __forceinline__ float hi2f(u32 v) { return __uint_as_float(v & 0xffff0000u); }
__device__ __forceinline__ u32 pk2(float lo, float hi) {
  u32 r; asm("v_cvt_pk_bf16_f32 %0, %1, %2" : "=v"(r) : "v"(lo), "v"(hi)); return r;
}
__device__ __forceinline__ float dot2(u32 a, u32 b, float c) {
  return __builtin_amdgcn_fdot2_f32_bf16(__builtin_bit_cast(bf16x2, a), __builtin_bit_cast(bf16x2, b), c, false);
}
__device__ __forceinline__ u32 plo(u32 w0, u32 w1) { return __builtin_amdgcn_perm(w1, w0, 0x05040100); }
__device__ __forceinline__ u32 phi(u32 w0, u32 w1) { return __builtin_amdgcn_perm(w1, w0, 0x07060302); }
template <int CTRL> __device__ __forceinline__ float dpp(float x) {
  return __builtin_bit_cast(float, __builtin_amdgcn_mov_dpp(__builtin_bit_cast(int, x), CTRL, 0xf, 0xf, true));
}
constexpr int XOR1 = 0xB1, XOR2 = 0x4E, XOR7 = 0x141, XOR8 = 0x128;
__device__ __forceinline__ float sw16sum(float a, float b) {
  auto s = __builtin_amdgcn_permlane16_swap(__float_as_uint(a), __float_as_uint(b), false, false);
  return __uint_as_float(s[0]) + __uint_as_float(s[1]);
}
__device__ __forceinline__ float sw32sum(float a, float b) {
  auto s = __builtin_amdgcn_permlane32_swap(__float_as_uint(a), __float_as_uint(b), false, false);
  return __uint_as_float(s[0]) + __uint_as_float(s[1]);
}
__device__ __forceinline__ float upper32(float a) {
  auto s = __builtin_amdgcn_permlane32_swap(__float_as_uint(a), __float_as_uint(a), false, false);
  return __uint_as_float(s[1]);
}
__device__ __forceinline__ float x1632max(float x) {
  auto s = __builtin_amdgcn_permlane16_swap(__float_as_uint(x), __float_as_uint(x), false, false);
  x = fmaxf(__uint_as_float(s[0]), __uint_as_float(s[1]));
  auto t = __builtin_amdgcn_permlane32_swap(__float_as_uint(x), __float_as_uint(x), false, false);
  return fmaxf(__uint_as_float(t[0]), __uint_as_float(t[1]));
}
__device__ __forceinline__ float x1632sum(float x) { x = sw16sum(x, x); return sw32sum(x, x); }
__device__ __forceinline__ float row16sum(float x) {
  x += dpp<XOR1>(x); x += dpp<XOR2>(x); x += dpp<XOR7>(x); x += dpp<XOR8>(x); return x;
}
__device__ __forceinline__ float wave_sum(float x) { return x1632sum(row16sum(x)); }
__device__ __forceinline__ float wave_max(float x) {
  x = fmaxf(x, dpp<XOR1>(x)); x = fmaxf(x, dpp<XOR2>(x)); x = fmaxf(x, dpp<XOR7>(x)); x = fmaxf(x, dpp<XOR8>(x));
  return x1632max(x);
}
__device__ __forceinline__ void st_gran(unsigned char* p, float v0, float v1, u32 ta, u32 tb) {
  u32x4 g = {__float_as_uint(v0), ta, __float_as_uint(v1), tb};
  asm volatile("global_store_dwordx4 %0, %1, off sc1\n\ts_nop 1" :: "v"(p), "v"(g) : "memory");
}
__device__ __forceinline__ void ld_gran1(const unsigned char* p, u32x4& a) {
  asm volatile("global_load_dwordx4 %0, %1, off sc1\n\ts_waitcnt vmcnt(0)" : "=&v"(a) : "v"(p) : "memory");
}
__device__ __forceinline__ void ld_gran2(const unsigned char* p, const unsigned char* q, u32x4& a, u32x4& b) {
  asm volatile("global_load_dwordx4 %0, %2, off sc1\n\tglobal_load_dwordx4 %1, %3, off sc1\n\ts_waitcnt vmcnt(0)"
               : "=&v"(a), "=&v"(b) : "v"(p), "v"(q) : "memory");
}
__device__ __forceinline__ void ld_gran3(const unsigned char* p0, const unsigned char* p1, const unsigned char* p2,
                                         u32x4& a, u32x4& b, u32x4& c) {
  asm volatile("global_load_dwordx4 %0, %3, off sc1\n\tglobal_load_dwordx4 %1, %4, off sc1\n\t"
               "global_load_dwordx4 %2, %5, off sc1\n\ts_waitcnt vmcnt(0)"
               : "=&v"(a), "=&v"(b), "=&v"(c) : "v"(p0), "v"(p1), "v"(p2) : "memory");
}
__device__ __forceinline__ void ld_gran10(const unsigned char* p0, const unsigned char* p0b, const unsigned char* p1,
                                          const unsigned char* p1b, u32x4 (&g)[10]) {
  asm volatile(
      "global_load_dwordx4 %0, %10, off sc1\n\tglobal_load_dwordx4 %1, %10, off offset:1536 sc1\n\t"
      "global_load_dwordx4 %2, %10, off offset:3072 sc1\n\tglobal_load_dwordx4 %3, %11, off sc1\n\t"
      "global_load_dwordx4 %4, %11, off offset:1536 sc1\n\t"
      "global_load_dwordx4 %5, %12, off sc1\n\tglobal_load_dwordx4 %6, %12, off offset:1536 sc1\n\t"
      "global_load_dwordx4 %7, %12, off offset:3072 sc1\n\tglobal_load_dwordx4 %8, %13, off sc1\n\t"
      "global_load_dwordx4 %9, %13, off offset:1536 sc1\n\ts_waitcnt vmcnt(0)"
      : "=&v"(g[0]), "=&v"(g[1]), "=&v"(g[2]), "=&v"(g[3]), "=&v"(g[4]), "=&v"(g[5]), "=&v"(g[6]), "=&v"(g[7]),
        "=&v"(g[8]), "=&v"(g[9])
      : "v"(p0), "v"(p0b), "v"(p1), "v"(p1b) : "memory");
}
__device__ __forceinline__ __amdgpu_buffer_rsrc_t mkrsrc(const void* p, u32 bytes) {
  return __builtin_amdgcn_make_buffer_rsrc(const_cast<void*>(p), 0, bytes, 0x00020000);
}
#define TAGOK(g) ((g)[1] == ta && (g)[3] == tb)
#define LDS_LD(p) __hip_atomic_load((p), __ATOMIC_RELAXED, __HIP_MEMORY_SCOPE_WORKGROUP)

#define STAMPT(t, i) do { } while (0)
#define STAMP(i) STAMPT(0, i)
struct Params {
  const uint16_t* x; const uint16_t* kc; const uint16_t* vc; const uint16_t* wq; const uint16_t* wo;
  const uint16_t* wg; const uint16_t* wu; const uint16_t* wd; uint16_t* out; unsigned char* ws;
};

constexpr int GUS = 324;
struct Lds {
  u32 wst[48 * GUS + 12 * 640];
  u32 xnp[2][320];
  float qpart[8][2][192];
  u32 qpk[96];
  float pl[9][64];
  float ml[10][2];
  float red[18][192];
  u32 opk[2][16];
  float wsum[8][16];
  float st[5][4];
  float hres[16];
  u32 hp[2][320];
  u32 gp[2][12];
  u32 qcnt, qflag, ocnt, pad1;
};

__global__ void __launch_bounds__(NT, 3) fused_kernel(Params P) {
  __shared__ __attribute__((aligned(16))) Lds S;
  const int tid = threadIdx.x, lane = tid & 63, wave = __builtin_amdgcn_readfirstlane(tid >> 6);
  const int half = lane >> 5, c = lane & 31;
  const int r4 = lane >> 4, c16 = lane & 15;
  const int wg = blockIdx.x, xcd = wg & 7, idx = wg >> 3;
  const int h = (idx >> 3) * 8 + xcd, j8 = idx & 7, b = j8 >> 2, sp = j8 & 3;
  const int prod = h * 8 + j8;
  const bool isF = sp == 3, isO = !isF;
  const int jr = j8 - b;
  const bool isQ = isO && jr < 5;
  const int fidx = h * 2 + b;
  const int rid = h * 5 + jr;
  const bool isR = isQ && rid < NRED;

  STAMP(0);
  if (tid == 0) { S.qcnt = 0u; S.qflag = 0u; S.ocnt = 0u; }
  __syncthreads();
  const unsigned long long ep = __hip_atomic_load((const unsigned long long*)(P.ws + WS_EPOCH), __ATOMIC_RELAXED, __HIP_MEMORY_SCOPE_AGENT);
  const u32 ta = (u32)ep + 1u, tb = (u32)(ep >> 32) + 1u;
  const u32 xres = ((const u32*)(P.x + ((tid >> 2) & 1) * D + (rid % NRED) * 8))[tid & 3];
  auto stream_body = [&](const int slotw, const int ng, const int row0, const bool waitq) {
    const size_t kvoff = ((size_t)(b * H + h) * SK + (SK - W) + sp * 544 + row0) * DH;
    const __amdgpu_buffer_rsrc_t krs = mkrsrc(P.kc + kvoff, ng * 1536);
    const __amdgpu_buffer_rsrc_t vrs = mkrsrc(P.vc + kvoff, ng * 1536);
    const u32 loA = r4 * 384 + c16 * 16, loB = r4 * 384 + 256 + c16 * 8;
    u32x4 ka[16]; u32x2 kb[16];
#pragma unroll
    for (int i = 0; i < 16; ++i) {
      ka[i] = __builtin_amdgcn_raw_buffer_load_b128(krs, loA, i * 1536, 2);
      kb[i] = __builtin_amdgcn_raw_buffer_load_b64(krs, loB, i * 1536, 2);
    }
    u32x4 va[16]; u32x2 vb[16];
#pragma unroll
    for (int i = 0; i < 8; ++i) {
      va[i] = __builtin_amdgcn_raw_buffer_load_b128(vrs, loA, i * 1536, 2);
      vb[i] = __builtin_amdgcn_raw_buffer_load_b64(vrs, loB, i * 1536, 2);
    }
    __builtin_amdgcn_sched_barrier(0);
    STAMP(1);
    if (waitq) { while (LDS_LD(&S.qflag) == 0u) __builtin_amdgcn_s_sleep(1); }
    asm volatile("" ::: "memory");
    STAMP(2);
    const u32x4 qa = *(const u32x4*)&S.qpk[4 * c16];
    const u32x2 qb = *(const u32x2*)&S.qpk[64 + 2 * c16];
    float d[16];
#pragma unroll
    for (int i = 0; i < 16; ++i) {
      float t = dot2(ka[i][0], qa[0], 0.f);
      t = dot2(ka[i][1], qa[1], t); t = dot2(ka[i][2], qa[2], t); t = dot2(ka[i][3], qa[3], t);
      t = dot2(kb[i][0], qb[0], t); d[i] = dot2(kb[i][1], qb[1], t);
    }
    __builtin_amdgcn_sched_barrier(0);
#pragma unroll
    for (int i = 8; i < 16; ++i) {
      va[i] = __builtin_amdgcn_raw_buffer_load_b128(vrs, loA, i * 1536, 2);
      vb[i] = __builtin_amdgcn_raw_buffer_load_b64(vrs, loB, i * 1536, 2);
    }
    __builtin_amdgcn_sched_barrier(0);
#define TSTEP(A, N, CTRL, BIT)                                                                 \
  _Pragma("unroll") for (int i = 0; i < N; ++i) {                                              \
    const bool hb = (c & BIT) != 0;                                                            \
    const float keep = hb ? A[i + N] : A[i], send = hb ? A[i] : A[i + N];                      \
    A[i] = keep + dpp<CTRL>(send);                                                             \
  }
    TSTEP(d, 8, XOR8, 8) TSTEP(d, 4, XOR7, 4) TSTEP(d, 2, XOR2, 2) TSTEP(d, 1, XOR1, 1)
    const float sc = c16 < ng ? rbf(rbf(d[0]) * 0.07216878364870322f) * 1.4426950408889634f : -INFINITY;
    const float mw = wave_max(sc);
    const float p = __builtin_amdgcn_exp2f(sc - mw);
    const float lw = wave_sum(p);
    S.pl[slotw][lane] = p;
    if (lane == 0) { S.ml[slotw][0] = mw; S.ml[slotw][1] = lw; }
    STAMP(3);
    asm volatile("s_waitcnt lgkmcnt(0)" ::: "memory");
    float acc[12];
#pragma unroll
    for (int k = 0; k < 12; ++k) acc[k] = 0.f;
#pragma unroll
    for (int i4 = 0; i4 < 4; ++i4) {
      const f32x4 pp = *(const f32x4*)&S.pl[slotw][r4 * 16 + i4 * 4];
#pragma unroll
      for (int j = 0; j < 4; ++j) {
        const u32x4 v = va[i4 * 4 + j]; const u32x2 w = vb[i4 * 4 + j];
#pragma unroll
        for (int k = 0; k < 4; ++k) {
          acc[2 * k] = fmaf(pp[j], lo2f(v[k]), acc[2 * k]);
          acc[2 * k + 1] = fmaf(pp[j], hi2f(v[k]), acc[2 * k + 1]);
        }
#pragma unroll
        for (int k = 0; k < 2; ++k) {
          acc[8 + 2 * k] = fmaf(pp[j], lo2f(w[k]), acc[8 + 2 * k]);
          acc[8 + 2 * k + 1] = fmaf(pp[j], hi2f(w[k]), acc[8 + 2 * k + 1]);
        }
      }
    }
#pragma unroll
    for (int k = 0; k < 6; ++k) acc[k] = sw32sum(acc[k], acc[k + 6]);
    float* dst = &S.red[slotw * 2 + (r4 & 1)][0];
    if (half == 0) {
      *(f32x4*)&dst[8 * c16] = f32x4{acc[0], acc[1], acc[2], acc[3]};
      *(f32x2*)&dst[8 * c16 + 4] = f32x2{acc[4], acc[5]};
    } else {
      *(f32x2*)&dst[8 * c16 + 6] = f32x2{acc[0], acc[1]};
      *(f32x4*)&dst[128 + 4 * c16] = f32x4{acc[2], acc[3], acc[4], acc[5]};
    }
    STAMP(4); STAMPT(448, 19); STAMPT(256, 20); STAMPT(704, 18);
  };

  if (wave == 11 && isO) {
    stream_body(8, 8, 512, true);
  } else if (wave >= 8) {
    __builtin_amdgcn_s_setprio(3);
    const int st = tid - 512;
    if (isQ) {
      u32 xr[2][5];
#pragma unroll
      for (int bb = 0; bb < 2; ++bb) {
        const u32* xp = (const u32*)(P.x + bb * D) + lane * 5;
#pragma unroll
        for (int i = 0; i < 5; ++i) xr[bb][i] = xp[i];
      }
      const int s2 = st < 192 ? st : 191, cc = s2 % 24, slot = s2 / 24;
      u32x4 wqr[16];
      {
        const __amdgpu_buffer_rsrc_t rs = mkrsrc(P.wq + (size_t)(jr * 128) * QN + h * DH, 128 * QN * 2);
        const u32 vo = (u32)(slot * 16) * (QN * 2) + cc * 16;
#pragma unroll
        for (int i = 0; i < 16; ++i) wqr[i] = __builtin_amdgcn_raw_buffer_load_b128(rs, vo, i * QN * 2, 2);
      }
#pragma unroll
      for (int bb = 0; bb < 2; ++bb) {
        float xv[10];
#pragma unroll
        for (int i = 0; i < 5; ++i) { xv[2 * i] = lo2f(xr[bb][i]); xv[2 * i + 1] = hi2f(xr[bb][i]); }
        float s = 0.f;
#pragma unroll
        for (int i = 0; i < 10; ++i) s += xv[i];
        const float mean = wave_sum(s) * (1.f / D);
        float q = 0.f;
#pragma unroll
        for (int i = 0; i < 10; ++i) { const float d = xv[i] - mean; q += d * d; }
        const float var = wave_sum(q) * (1.f / D);
        const float mb = rbf(mean), den = rbf(sqrtf(rbf(rbf(var) + 1e-5f)));
#pragma unroll
        for (int i = 0; i < 5; ++i) S.xnp[bb][lane * 5 + i] = pk2(rbf(xv[2 * i] - mb) / den, rbf(xv[2 * i + 1] - mb) / den);
      }
      asm volatile("s_waitcnt lgkmcnt(0)" ::: "memory");
      float a[2][8];
#pragma unroll
      for (int k = 0; k < 8; ++k) { a[0][k] = 0.f; a[1][k] = 0.f; }
#pragma unroll
      for (int i4 = 0; i4 < 2; ++i4) {
        const u32x4 x0 = *(const u32x4*)&S.xnp[0][jr * 64 + slot * 8 + i4 * 4];
        const u32x4 x1 = *(const u32x4*)&S.xnp[1][jr * 64 + slot * 8 + i4 * 4];
#pragma unroll
        for (int i = 0; i < 4; ++i) {
          const u32x4 w0 = wqr[(i4 * 4 + i) * 2], w1 = wqr[(i4 * 4 + i) * 2 + 1];
#pragma unroll
          for (int k = 0; k < 4; ++k) {
            const u32 wl = plo(w0[k], w1[k]), wh = phi(w0[k], w1[k]);
            a[0][2 * k] = dot2(wl, x0[i], a[0][2 * k]); a[0][2 * k + 1] = dot2(wh, x0[i], a[0][2 * k + 1]);
            a[1][2 * k] = dot2(wl, x1[i], a[1][2 * k]); a[1][2 * k + 1] = dot2(wh, x1[i], a[1][2 * k + 1]);
          }
        }
      }
      if (st < 192) {
#pragma unroll
        for (int bb = 0; bb < 2; ++bb) {
          *(f32x4*)&S.qpart[slot][bb][cc * 8] = f32x4{a[bb][0], a[bb][1], a[bb][2], a[bb][3]};
          *(f32x4*)&S.qpart[slot][bb][cc * 8 + 4] = f32x4{a[bb][4], a[bb][5], a[bb][6], a[bb][7]};
        }
      }
      asm volatile("s_waitcnt lgkmcnt(0)" ::: "memory");
      if (lane == 0) __hip_atomic_fetch_add(&S.qcnt, 1u, __ATOMIC_RELAXED, __HIP_MEMORY_SCOPE_WORKGROUP);
      while (LDS_LD(&S.qcnt) != 3u) __builtin_amdgcn_s_sleep(1);
      asm volatile("" ::: "memory");
      STAMPT(512, 14);
      if (st < 192) {
        const int bb = st / 96, k = st % 96;
        float s0 = 0.f, s1 = 0.f;
#pragma unroll
        for (int w = 0; w < 8; ++w) { const f32x2 t = *(const f32x2*)&S.qpart[w][bb][2 * k]; s0 += t[0]; s1 += t[1]; }
        st_gran(P.ws + WS_Q + ((size_t)((h * 2 + bb) * 5 + jr) * 96 + k) * 16, s0, s1, ta, tb);
      }
    }
    if (wave == 8) {
      const unsigned char* pq = P.ws + WS_Q + (size_t)(h * 2 + b) * 480 * 16;
      const unsigned char* p0 = pq + lane * 16;
      const unsigned char* p1 = pq + (64 + c) * 16;
      u32x4 g[10];
      for (int it = 0; it < SPIN_LIMIT; ++it) {
        ld_gran10(p0, p0 + 4608, p1, p1 + 4608, g);
        bool ok = true;
#pragma unroll
        for (int i = 0; i < 10; ++i) ok = ok && TAGOK(g[i]);
        if (__all(ok)) break;
        __builtin_amdgcn_s_sleep(2);
      }
      STAMPT(512, 15);
      float s0 = 0.f, s1 = 0.f, t0 = 0.f, t1 = 0.f;
#pragma unroll
      for (int i = 0; i < 5; ++i) {
        s0 += __uint_as_float(g[i][0]); s1 += __uint_as_float(g[i][2]);
        t0 += __uint_as_float(g[5 + i][0]); t1 += __uint_as_float(g[5 + i][2]);
      }
      S.qpk[lane] = pk2(s0, s1);
      if (lane < 32) S.qpk[64 + lane] = pk2(t0, t1);
      asm volatile("s_waitcnt lgkmcnt(0)" ::: "memory");
      if (lane == 0) __hip_atomic_store(&S.qflag, 1u, __ATOMIC_RELAXED, __HIP_MEMORY_SCOPE_WORKGROUP);
    } else {
      __builtin_amdgcn_s_setprio(3);
      const int tw = st - 64;
      if (isO) {
        const __amdgpu_buffer_rsrc_t ro = mkrsrc(P.wo + (size_t)(h * DH + jr * 32) * D, 32 * D * 2);
        u32x4 w0[10], w1[10];
#pragma unroll
        for (int i = 0; i < 10; ++i) {
          const int task = tw + 128 * i, k = task / 80, ch = task % 80;
          w0[i] = __builtin_amdgcn_raw_buffer_load_b128(ro, (u32)(2 * k) * (D * 2) + ch * 16, 0, 2);
          w1[i] = __builtin_amdgcn_raw_buffer_load_b128(ro, (u32)(2 * k) * (D * 2) + ch * 16, D * 2, 2);
        }
#pragma unroll
        for (int i = 0; i < 10; ++i) {
          const int task = tw + 128 * i, k = task / 80, ch = task % 80;
          u32* d = &S.wst[k * 640 + ch * 8];
          *(u32x4*)d = u32x4{plo(w0[i][0], w1[i][0]), phi(w0[i][0], w1[i][0]), plo(w0[i][1], w1[i][1]), phi(w0[i][1], w1[i][1])};
          *(u32x4*)(d + 4) = u32x4{plo(w0[i][2], w1[i][2]), phi(w0[i][2], w1[i][2]), plo(w0[i][3], w1[i][3]), phi(w0[i][3], w1[i][3])};
        }
      } else {
        {
          const __amdgpu_buffer_rsrc_t rg = mkrsrc(P.wg + fidx * 24, (D * F - fidx * 24) * 2);
          u32x4 a0[5], a1[5];
#pragma unroll
          for (int i = 0; i < 5; ++i) {
            const int task = tw + 192 * i, k = task / 3, q = task % 3;
            const u32 vo = (u32)(2 * k) * (F * 2) + q * 16;
            a0[i] = __builtin_amdgcn_raw_buffer_load_b128(rg, vo, 0, GUAUX);
            a1[i] = __builtin_amdgcn_raw_buffer_load_b128(rg, vo, F * 2, GUAUX);
          }
#pragma unroll
          for (int i = 0; i < 5; ++i) {
            const int task = tw + 192 * i, k = task / 3, q = task % 3;
#pragma unroll
            for (int e = 0; e < 4; ++e) {
              S.wst[(q * 8 + 2 * e) * GUS + k] = plo(a0[i][e], a1[i][e]);
              S.wst[(q * 8 + 2 * e + 1) * GUS + k] = phi(a0[i][e], a1[i][e]);
            }
          }
        }
        const __amdgpu_buffer_rsrc_t rd = mkrsrc(P.wd + (size_t)(fidx * 24) * D, 24 * D * 2);
        u32x4 w0[5], w1[5];
#pragma unroll
        for (int i = 0; i < 5; ++i) {
          const int task = tw + 192 * i, k = task / 80, ch = task % 80;
          w0[i] = __builtin_amdgcn_raw_buffer_load_b128(rd, (u32)(2 * k) * (D * 2) + ch * 16, 0, 2);
          w1[i] = __builtin_amdgcn_raw_buffer_load_b128(rd, (u32)(2 * k) * (D * 2) + ch * 16, D * 2, 2);
        }
#pragma unroll
        for (int i = 0; i < 5; ++i) {
          const int task = tw + 192 * i, k = task / 80, ch = task % 80;
          u32* d = &S.wst[48 * GUS + k * 640 + ch * 8];
          *(u32x4*)d = u32x4{plo(w0[i][0], w1[i][0]), phi(w0[i][0], w1[i][0]), plo(w0[i][1], w1[i][1]), phi(w0[i][1], w1[i][1])};
          *(u32x4*)(d + 4) = u32x4{plo(w0[i][2], w1[i][2]), phi(w0[i][2], w1[i][2]), plo(w0[i][3], w1[i][3]), phi(w0[i][3], w1[i][3])};
        }
      }
      STAMPT(576, 16);
    }
  } else {
    stream_body(wave, isF ? 13 : 16, isF ? wave * 52 : wave * 64, true);
  }
  __syncthreads();
  const int t3 = tid < 320 ? tid : 319;
  u32x2 wpre[16];
  if (isO) {
#pragma unroll
    for (int k = 0; k < 16; ++k) wpre[k] = *(const u32x2*)&S.wst[k * 640 + 2 * t3];
  }
  if (tid < 128) {
    float mm[9];
    float Mwg = -INFINITY;
#pragma unroll
    for (int w = 0; w < 9; ++w) { mm[w] = (w < 8 || isO) ? S.ml[w][0] : -INFINITY; Mwg = fmaxf(Mwg, mm[w]); }
    float Lwg = 0.f;
#pragma unroll
    for (int w = 0; w < 9; ++w) { mm[w] = __builtin_amdgcn_exp2f(mm[w] - Mwg); Lwg = fmaf(mm[w], (w < 8 || isO) ? S.ml[w][1] : 0.f, Lwg); }
    const int t = tid < 97 ? (tid > 0 ? tid - 1 : 0) : 95;
    float o0 = 0.f, o1 = 0.f;
#pragma unroll
    for (int w = 0; w < 9; ++w) {
      if (w < 8 || isO) {
        const f32x2 a = *(const f32x2*)&S.red[2 * w][2 * t], bb = *(const f32x2*)&S.red[2 * w + 1][2 * t];
        o0 = fmaf(mm[w], a[0] + bb[0], o0); o1 = fmaf(mm[w], a[1] + bb[1], o1);
      }
    }
    if (tid == 0) { o0 = Mwg; o1 = Lwg; }
    if (tid < 97) st_gran(P.ws + WS_O + ((size_t)prod * 128 + tid) * 16, o0, o1, ta, tb);
    if (lane == 0) __hip_atomic_fetch_add(&S.ocnt, 1u, __ATOMIC_RELAXED, __HIP_MEMORY_SCOPE_WORKGROUP);
    STAMP(5);
    if (isO) {
      const unsigned char* pb = P.ws + WS_O + (size_t)(h * 8 + wave * 4 + r4) * 128 * 16;
      u32x4 go, gm;
      for (int it = 0; it < SPIN_LIMIT; ++it) {
        ld_gran2(pb + (1 + jr * 16 + c16) * 16, pb, go, gm);
        if (__all(TAGOK(go) && TAGOK(gm))) break;
        __builtin_amdgcn_s_sleep(1);
      }
      const float m = __uint_as_float(gm[0]), l = __uint_as_float(gm[2]);
      const float M = x1632max(m);
      const float e = __builtin_amdgcn_exp2f(m - M);
      const float L = x1632sum(e * l);
      const float a0 = x1632sum(e * __uint_as_float(go[0])), a1 = x1632sum(e * __uint_as_float(go[2]));
      const float rl = __builtin_amdgcn_rcpf(L);
      if (lane < 16) S.opk[wave][lane] = pk2(a0 * rl, a1 * rl);
    }
  }
  const int gslot = (tid >> 2) * 8 + (tid & 3);
  if (isO) {
    __syncthreads();
    STAMP(6);
    if (tid < 320) {
      float y00 = 0.f, y01 = 0.f, y10 = 0.f, y11 = 0.f;
#pragma unroll
      for (int k4 = 0; k4 < 4; ++k4) {
        const u32x4 oa = *(const u32x4*)&S.opk[0][k4 * 4], ob = *(const u32x4*)&S.opk[1][k4 * 4];
#pragma unroll
        for (int k = 0; k < 4; ++k) {
          const u32x2 w = wpre[k4 * 4 + k];
          y00 = dot2(w[0], oa[k], y00); y01 = dot2(w[1], oa[k], y01);
          y10 = dot2(w[0], ob[k], y10); y11 = dot2(w[1], ob[k], y11);
        }
      }
      unsigned char* py = P.ws + WS_Y + ((size_t)(h * 6 + jr) * 640 + gslot) * 16;
      st_gran(py, y00, y01, ta, tb);
      st_gran(py + 64, y10, y11, ta, tb);
    }
    STAMP(7);
  }
  if (isR) {
    if (tid < 512) {
      const unsigned char* p0 = P.ws + WS_Y + ((size_t)(tid >> 3) * 640 + rid * 8 + (tid & 7)) * 16;
      constexpr size_t ST = (size_t)64 * 640 * 16;
      u32x4 ga, gb, gc;
      for (int it = 0; it < SPIN_LIMIT; ++it) {
        ld_gran3(p0, p0 + ST, p0 + 2 * ST, ga, gb, gc);
        if (__all(TAGOK(ga) && TAGOK(gb) && TAGOK(gc))) break;
        __builtin_amdgcn_s_sleep(1);
      }
      STAMP(8);
      float y0 = __uint_as_float(ga[0]) + __uint_as_float(gb[0]) + __uint_as_float(gc[0]);
      float y1 = __uint_as_float(ga[2]) + __uint_as_float(gb[2]) + __uint_as_float(gc[2]);
      y0 += dpp<XOR8>(y0); y1 += dpp<XOR8>(y1);
      y0 = x1632sum(y0); y1 = x1632sum(y1);
      if (lane < 8) *(f32x2*)&S.wsum[wave][lane * 2] = f32x2{y0, y1};
    }
    __syncthreads();
    if (tid < 8) {
      float a0 = 0.f, a1 = 0.f;
#pragma unroll
      for (int w = 0; w < 8; ++w) { a0 += S.wsum[w][tid * 2]; a1 += S.wsum[w][tid * 2 + 1]; }
      const float h0 = rbf(lo2f(xres) + rbf(a0)), h1 = rbf(hi2f(xres) + rbf(a1));
      S.hres[tid * 2] = h0; S.hres[tid * 2 + 1] = h1;
      st_gran(P.ws + WS_H + (size_t)(rid * 8 + tid) * 16, h0, h1, ta, tb);
    }
    STAMP(9);
  }
  if (isF) {
    if (wave >= 8) {
      while (LDS_LD(&S.ocnt) != 2u) __builtin_amdgcn_s_sleep(1);
      asm volatile("" ::: "memory");
      const int st = tid - 512;
      const __amdgpu_buffer_rsrc_t ru = mkrsrc(P.wu + fidx * 24, (D * F - fidx * 24) * 2);
      u32x4 a0[4], a1[4];
#pragma unroll
      for (int i = 0; i < 4; ++i) {
        const int task = st + 256 * i, k = task / 3, q = task % 3;
        const u32 vo = task < 960 ? (u32)(2 * k) * (F * 2) + q * 16 : 0x7fffff00u;
        a0[i] = __builtin_amdgcn_raw_buffer_load_b128(ru, vo, 0, GUAUX);
        a1[i] = __builtin_amdgcn_raw_buffer_load_b128(ru, vo, F * 2, GUAUX);
      }
#pragma unroll
      for (int i = 0; i < 4; ++i) {
        const int task = st + 256 * i, k = task / 3, q = task % 3;
        if (task < 960) {
#pragma unroll
          for (int e = 0; e < 4; ++e) {
            S.wst[(24 + q * 8 + 2 * e) * GUS + k] = plo(a0[i][e], a1[i][e]);
            S.wst[(24 + q * 8 + 2 * e + 1) * GUS + k] = phi(a0[i][e], a1[i][e]);
          }
        }
      }
      STAMPT(512, 17);
    }
    __syncthreads();
    u32x4 wreg[5];
    {
      const u32* wrow = &S.wst[((r4 >> 1) * 24 + 2 * wave + (r4 & 1)) * GUS + 20 * c16];
#pragma unroll
      for (int i = 0; i < 5; ++i) wreg[i] = *(const u32x4*)&wrow[4 * i];
#pragma unroll
      for (int k = 0; k < 12; ++k) wpre[k] = *(const u32x2*)&S.wst[48 * GUS + k * 640 + 2 * t3];
    }
    float cs = 0.f;
#pragma unroll
    for (int i = 0; i < 5; ++i)
#pragma unroll
      for (int k = 0; k < 4; ++k) cs = dot2(wreg[i][k], 0x3f803f80u, cs);
    cs = row16sum(cs);
    if (tid < 320) {
      const unsigned char* p0 = P.ws + WS_H + (size_t)gslot * 16;
      u32x4 ga, gb;
      for (int it = 0; it < SPIN_LIMIT; ++it) {
        ld_gran2(p0, p0 + 64, ga, gb);
        if (__all(TAGOK(ga) && TAGOK(gb))) break;
        __builtin_amdgcn_s_sleep(1);
      }
      STAMP(10);
      const float h00 = __uint_as_float(ga[0]), h01 = __uint_as_float(ga[2]), h10 = __uint_as_float(gb[0]), h11 = __uint_as_float(gb[2]);
      S.hp[0][tid] = (ga[0] >> 16) | (ga[2] & 0xffff0000u);
      S.hp[1][tid] = (gb[0] >> 16) | (gb[2] & 0xffff0000u);
      float s0 = h00 + h01, q0 = h00 * h00 + h01 * h01, s1 = h10 + h11, q1 = h10 * h10 + h11 * h11;
      s0 = sw32sum(s0, s1); q0 = sw32sum(q0, q1);
      s0 = sw16sum(s0, q0);
      s0 = row16sum(s0);
      if (c16 == 0) S.st[wave][r4] = s0;
    }
    __syncthreads();
    {
      const int bsel = c16 & 1;
      float d0 = 0.f, d1 = 0.f;
#pragma unroll
      for (int i = 0; i < 5; ++i) {
        const u32x4 ha = *(const u32x4*)&S.hp[0][20 * c16 + 4 * i], hb = *(const u32x4*)&S.hp[1][20 * c16 + 4 * i];
#pragma unroll
        for (int k = 0; k < 4; ++k) { d0 = dot2(wreg[i][k], ha[k], d0); d1 = dot2(wreg[i][k], hb[k], d1); }
      }
      float sm = 0.f, sq = 0.f;
#pragma unroll
      for (int w = 0; w < 5; ++w) { const f32x2 t = *(const f32x2*)&S.st[w][bsel * 2]; sm += t[0]; sq += t[1]; }
      const float mean = sm * (1.f / D);
      const float var = fmaxf(sq * (1.f / D) - mean * mean, 0.f);
      const float mb = rbf(mean), den = rbf(__builtin_amdgcn_sqrtf(rbf(rbf(var) + 1e-5f)));
      const float rden = __builtin_amdgcn_rcpf(den);
      float dd = (bsel ? d1 : d0) + dpp<XOR1>(bsel ? d0 : d1);
      dd += dpp<XOR2>(dd); dd += dpp<XOR1>(dpp<XOR7>(dd)); dd += dpp<XOR8>(dd);
      const float mine = rbf((dd - mb * cs) * rden);
      const float up = upper32(mine);
      const float t = 0.7978845608028654f * (mine + 0.044715f * mine * mine * mine);
      const float th = 1.f - 2.f * __builtin_amdgcn_rcpf(__builtin_amdgcn_exp2f(t * 2.8853900817779268f) + 1.f);
      const float gval = rbf(rbf(0.5f * mine * (1.f + th)) * up);
      if (half == 0 && c16 < 2) ((uint16_t*)&S.gp[c16][0])[2 * wave + r4] = (uint16_t)(__float_as_uint(gval) >> 16);
    }
    __syncthreads();
    if (tid < 320) {
      float z00 = 0.f, z01 = 0.f, z10 = 0.f, z11 = 0.f;
#pragma unroll
      for (int k4 = 0; k4 < 3; ++k4) {
        const u32x4 oa = *(const u32x4*)&S.gp[0][k4 * 4], ob = *(const u32x4*)&S.gp[1][k4 * 4];
#pragma unroll
        for (int k = 0; k < 4; ++k) {
          const u32x2 w = wpre[k4 * 4 + k];
          z00 = dot2(w[0], oa[k], z00); z01 = dot2(w[1], oa[k], z01);
          z10 = dot2(w[0], ob[k], z10); z11 = dot2(w[1], ob[k], z11);
        }
      }
      unsigned char* pz = P.ws + WS_Z + ((size_t)fidx * 640 + gslot) * 16;
      st_gran(pz, z00, z01, ta, tb);
      st_gran(pz + 64, z10, z11, ta, tb);
    }
    STAMP(11);
  }
  if (isR) {
    float y0 = 0.f, y1 = 0.f;
    if (tid < 512) {
      const unsigned char* p0 = P.ws + WS_Z + ((size_t)(tid >> 3) * 640 + rid * 8 + (tid & 7)) * 16;
      u32x4 ga;
      for (int it = 0; it < SPIN_LIMIT; ++it) {
        ld_gran1(p0, ga);
        if (__all(TAGOK(ga))) break;
        __builtin_amdgcn_s_sleep(1);
      }
      STAMP(12);
      y0 = __uint_as_float(ga[0]); y1 = __uint_as_float(ga[2]);
      y0 += dpp<XOR8>(y0); y1 += dpp<XOR8>(y1);
      y0 = x1632sum(y0); y1 = x1632sum(y1);
      if (lane < 8) *(f32x2*)&S.red[wave][lane * 2] = f32x2{y0, y1};
    }
    __syncthreads();
    if (tid < 8) {
      float a0 = 0.f, a1 = 0.f;
#pragma unroll
      for (int w = 0; w < 8; ++w) { a0 += S.red[w][tid * 2]; a1 += S.red[w][tid * 2 + 1]; }
      const u32 o = pk2(S.hres[tid * 2] + rbf(a0), S.hres[tid * 2 + 1] + rbf(a1));
      ((u32*)(P.out + (tid >> 2) * D + rid * 8))[tid & 3] = o;
    }
    if (wg == 0 && tid == 0) {
      u32x2 ne = {ta, tb};
      asm volatile("global_store_dwordx2 %0, %1, off sc1" :: "v"(P.ws + WS_EPOCH), "v"(ne) : "memory");
    }
    STAMP(13);
  }
}
}

extern "C" void kernel_launch(void* const* d_in, const int* in_sizes, int n_in, void* d_out, int out_size,
                              void* d_ws, size_t ws_size, hipStream_t stream) {
  Params P;
  P.x = (const uint16_t*)d_in[0]; P.kc = (const uint16_t*)d_in[1]; P.vc = (const uint16_t*)d_in[2];
  P.wq = (const uint16_t*)d_in[3]; P.wo = (const uint16_t*)d_in[4];
  P.wg = (const uint16_t*)d_in[6]; P.wu = (const uint16_t*)d_in[7]; P.wd = (const uint16_t*)d_in[8];
  P.out = (uint16_t*)d_out; P.ws = (unsigned char*)d_ws;
  fused_kernel<<<NWG, NT, 0, stream>>>(P);
}
```

```cpp
#include <hip/hip_runtime.h>
#include <hip/hip_bf16.h>
#include <stdint.h>

namespace {
typedef unsigned u32;
typedef u32 u32x2 __attribute__((ext_vector_type(2)));
typedef u32 u32x4 __attribute__((ext_vector_type(4)));
typedef float f32x2 __attribute__((ext_vector_type(2)));
typedef float f32x4 __attribute__((ext_vector_type(4)));
typedef __bf16 bf16x2 __attribute__((ext_vector_type(2)));

constexpr int D = 640, H = 32, SK = 8192, DH = 192, W = 2048, F = 1536, QN = H * DH;
constexpr int NWG = 256, NT = 768, NRED = 80;
#define PACED 0
#define PWIN 2
#define GUAUX 0
#define YSLEEP 1
#define GMODE 0
constexpr int SPIN_LIMIT = 1 << 20;

constexpr size_t WS_EPOCH = 0;
constexpr size_t WS_Q = 4096;
constexpr size_t WS_O = WS_Q + (size_t)32 * 2 * 480 * 16;
constexpr size_t WS_H = WS_O + (size_t)256 * 128 * 16;
constexpr size_t WS_Y = WS_H + 640 * 16;
constexpr size_t WS_Z = WS_Y + (size_t)192 * 640 * 16;
constexpr size_t WS_END = WS_Z + (size_t)64 * 640 * 16;

__device__ __forceinline__ float rbf(float f) { return __bfloat162float(__float2bfloat16(f)); }
__device__ __forceinline__ float lo2f(u32 v) { return __uint_as_float(v << 16); }
__device__ __forceinline__ float hi2f(u32 v) { return __uint_as_float(v & 0xffff0000u); }
__device__ __forceinline__ u32 pk2(float lo, float hi) {
  u32 r; asm("v_cvt_pk_bf16_f32 %0, %1, %2" : "=v"(r) : "v"(lo), "v"(hi)); return r;
}
__device__ __forceinline__ float dot2(u32 a, u32 b, float c) {
  return __builtin_amdgcn_fdot2_f32_bf16(__builtin_bit_cast(bf16x2, a), __builtin_bit_cast(bf16x2, b), c, false);
}
__device__ __forceinline__ u32 plo(u32 w0, u32 w1) { return __builtin_amdgcn_perm(w1, w0, 0x05040100); }
__device__ __forceinline__ u32 phi(u32 w0, u32 w1) { return __builtin_amdgcn_perm(w1, w0, 0x07060302); }
template <int CTRL> __device__ __forceinline__ float dpp(float x) {
  return __builtin_bit_cast(float, __builtin_amdgcn_mov_dpp(__builtin_bit_cast(int, x), CTRL, 0xf, 0xf, true));
}
constexpr int XOR1 = 0xB1, XOR2 = 0x4E, XOR7 = 0x141, XOR8 = 0x128;
__device__ __forceinline__ float sw16sum(float a, float b) {
  auto s = __builtin_amdgcn_permlane16_swap(__float_as_uint(a), __float_as_uint(b), false, false);
  return __uint_as_float(s[0]) + __uint_as_float(s[1]);
}
__device__ __forceinline__ float sw32sum(float a, float b) {
  auto s = __builtin_amdgcn_permlane32_swap(__float_as_uint(a), __float_as_uint(b), false, false);
  return __uint_as_float(s[0]) + __uint_as_float(s[1]);
}
__device__ __forceinline__ float upper32(float a) {
  auto s = __builtin_amdgcn_permlane32_swap(__float_as_uint(a), __float_as_uint(a), false, false);
  return __uint_as_float(s[1]);
}
__device__ __forceinline__ float x1632max(float x) {
  auto s = __builtin_amdgcn_permlane16_swap(__float_as_uint(x), __float_as_uint(x), false, false);
  x = fmaxf(__uint_as_float(s[0]), __uint_as_float(s[1]));
  auto t = __builtin_amdgcn_permlane32_swap(__float_as_uint(x), __float_as_uint(x), false, false);
  return fmaxf(__uint_as_float(t[0]), __uint_as_float(t[1]));
}
__device__ __forceinline__ float x1632sum(float x) { x = sw16sum(x, x); return sw32sum(x, x); }
__device__ __forceinline__ float row16sum(float x) {
  x += dpp<XOR1>(x); x += dpp<XOR2>(x); x += dpp<XOR7>(x); x += dpp<XOR8>(x); return x;
}
__device__ __forceinline__ float wave_sum(float x) { return x1632sum(row16sum(x)); }
__device__ __forceinline__ float wave_max(float x) {
  x = fmaxf(x, dpp<XOR1>(x)); x = fmaxf(x, dpp<XOR2>(x)); x = fmaxf(x, dpp<XOR7>(x)); x = fmaxf(x, dpp<XOR8>(x));
  return x1632max(x);
}
__device__ __forceinline__ void st_gran(unsigned char* p, float v0, float v1, u32 ta, u32 tb) {
  u32x4 g = {__float_as_uint(v0), ta, __float_as_uint(v1), tb};
  asm volatile("global_store_dwordx4 %0, %1, off sc1\n\ts_nop 1" :: "v"(p), "v"(g) : "memory");
}
__device__ __forceinline__ void ld_gran1(const unsigned char* p, u32x4& a) {
  asm volatile("global_load_dwordx4 %0, %1, off sc1\n\ts_waitcnt vmcnt(0)" : "=&v"(a) : "v"(p) : "memory");
}
__device__ __forceinline__ void ld_gran2(const unsigned char* p, const unsigned char* q, u32x4& a, u32x4& b) {
  asm volatile("global_load_dwordx4 %0, %2, off sc1\n\tglobal_load_dwordx4 %1, %3, off sc1\n\ts_waitcnt vmcnt(0)"
               : "=&v"(a), "=&v"(b) : "v"(p), "v"(q) : "memory");
}
__device__ __forceinline__ void ld_gran3(const unsigned char* p0, const unsigned char* p1, const unsigned char* p2,
                                         u32x4& a, u32x4& b, u32x4& c) {
  asm volatile("global_load_dwordx4 %0, %3, off sc1\n\tglobal_load_dwordx4 %1, %4, off sc1\n\t"
               "global_load_dwordx4 %2, %5, off sc1\n\ts_waitcnt vmcnt(0)"
               : "=&v"(a), "=&v"(b), "=&v"(c) : "v"(p0), "v"(p1), "v"(p2) : "memory");
}
__device__ __forceinline__ void ld_gran4(const unsigned char* p0, const unsigned char* p1, const unsigned char* p2,
                                         const unsigned char* p3, u32x4& a, u32x4& b, u32x4& c, u32x4& d) {
  asm volatile("global_load_dwordx4 %0, %4, off sc1\n\tglobal_load_dwordx4 %1, %5, off sc1\n\t"
               "global_load_dwordx4 %2, %6, off sc1\n\tglobal_load_dwordx4 %3, %7, off sc1\n\ts_waitcnt vmcnt(0)"
               : "=&v"(a), "=&v"(b), "=&v"(c), "=&v"(d) : "v"(p0), "v"(p1), "v"(p2), "v"(p3) : "memory");
}
__device__ __forceinline__ void ld_gran10(const unsigned char* p0, const unsigned char* p0b, const unsigned char* p1,
                                          const unsigned char* p1b, u32x4 (&g)[10]) {
  asm volatile(
      "global_load_dwordx4 %0, %10, off sc1\n\tglobal_load_dwordx4 %1, %10, off offset:1536 sc1\n\t"
      "global_load_dwordx4 %2, %10, off offset:3072 sc1\n\tglobal_load_dwordx4 %3, %11, off sc1\n\t"
      "global_load_dwordx4 %4, %11, off offset:1536 sc1\n\t"
      "global_load_dwordx4 %5, %12, off sc1\n\tglobal_load_dwordx4 %6, %12, off offset:1536 sc1\n\t"
      "global_load_dwordx4 %7, %12, off offset:3072 sc1\n\tglobal_load_dwordx4 %8, %13, off sc1\n\t"
      "global_load_dwordx4 %9, %13, off offset:1536 sc1\n\ts_waitcnt vmcnt(0)"
      : "=&v"(g[0]), "=&v"(g[1]), "=&v"(g[2]), "=&v"(g[3]), "=&v"(g[4]), "=&v"(g[5]), "=&v"(g[6]), "=&v"(g[7]),
        "=&v"(g[8]), "=&v"(g[9])
      : "v"(p0), "v"(p0b), "v"(p1), "v"(p1b) : "memory");
}
__device__ __forceinline__ __amdgpu_buffer_rsrc_t mkrsrc(const void* p, u32 bytes) {
  return __builtin_amdgcn_make_buffer_rsrc(const_cast<void*>(p), 0, bytes, 0x00020000);
}
#define TAGOK(g) ((g)[1] == ta && (g)[3] == tb)
#define LDS_LD(p) __hip_atomic_load((p), __ATOMIC_RELAXED, __HIP_MEMORY_SCOPE_WORKGROUP)

#define STAMPT(t, i) do { } while (0)
#define STAMP(i) STAMPT(0, i)
struct Params {
  const uint16_t* x; const uint16_t* kc; const uint16_t* vc; const uint16_t* wq; const uint16_t* wo;
  const uint16_t* wg; const uint16_t* wu; const uint16_t* wd; uint16_t* out; unsigned char* ws;
};

constexpr int GUS = 324;
struct Lds {
  u32 wst[48 * GUS + 12 * 640];
  u32 xnp[2][320];
  float qpart[8][2][192];
  u32 qpk[96];
  float pl[9][64];
  float ml[10][2];
  float red[18][192];
  u32 opk[2][16];
  float wsum[8][16];
  float st[5][4];
  float hres[16];
  u32 hp[2][320];
  u32 gp[2][12];
  float oown[200];
  float zsum[8][16];
  u32 qcnt, qflag, ocnt, oflag, hcnt, zcnt, ycnt, hflag;
};

__global__ void __launch_bounds__(NT, 3) fused_kernel(Params P) {
  __shared__ __attribute__((aligned(16))) Lds S;
  const int tid = threadIdx.x, lane = tid & 63, wave = __builtin_amdgcn_readfirstlane(tid >> 6);
  const int half = lane >> 5, c = lane & 31;
  const int r4 = lane >> 4, c16 = lane & 15;
  const int wg = blockIdx.x, xcd = wg & 7, idx = wg >> 3;
  const int h = (idx >> 3) * 8 + xcd, j8 = idx & 7, b = j8 >> 2, sp = j8 & 3;
  const int prod = h * 8 + j8;
  const bool isF = sp == 3, isO = !isF;
  const int jr = j8 - b;
  const bool isQ = isO && jr < 5;
  const int fidx = xcd * 8 + (idx >> 3) * 2 + b;
  const int rid = h * 5 + jr;
  const bool isR = isQ && rid < NRED;

  STAMP(0);
  if (tid == 0) { S.qcnt = 0u; S.qflag = 0u; S.ocnt = 0u; S.oflag = 0u; S.hcnt = 0u; S.zcnt = 0u; S.ycnt = 0u; S.hflag = 0u; }
  __syncthreads();
  if (wave >= 8 && !(wave == 11 && isO)) __builtin_amdgcn_s_setprio(3);
  unsigned long long ep = __hip_atomic_load((const unsigned long long*)(P.ws + WS_EPOCH), __ATOMIC_RELAXED, __HIP_MEMORY_SCOPE_AGENT);
  u32 ta, tb;
#define GET_TAGS() do { asm volatile("" : "+v"(ep)); ta = (u32)ep + 1u; tb = (u32)(ep >> 32) + 1u; } while (0)
  const u32 xres = ((const u32*)(P.x + ((tid >> 2) & 1) * D + (rid % NRED) * 8))[tid & 3];
  auto stream_body = [&](const int slotw, const int ng, const int row0, const bool waitq) {
    const size_t kvoff = ((size_t)(b * H + h) * SK + (SK - W) + sp * 544 + row0) * DH;
    const __amdgpu_buffer_rsrc_t krs = mkrsrc(P.kc + kvoff, ng * 1536);
    const __amdgpu_buffer_rsrc_t vrs = mkrsrc(P.vc + kvoff, ng * 1536);
    const u32 loA = r4 * 384 + c16 * 16, loB = r4 * 384 + 256 + c16 * 8;
    u32x4 ka[16]; u32x2 kb[16];
#pragma unroll
    for (int i = 0; i < 16; ++i) {
      ka[i] = __builtin_amdgcn_raw_buffer_load_b128(krs, loA, i * 1536, 2);
      kb[i] = __builtin_amdgcn_raw_buffer_load_b64(krs, loB, i * 1536, 2);
      if (i >= PWIN - 1 && i < PACED) {
        __builtin_amdgcn_sched_barrier(0);
        if (LDS_LD(&S.qflag) == 0u) asm volatile("s_waitcnt vmcnt(%0)" :: "i"(2 * (PWIN - 1)) : "memory");
        __builtin_amdgcn_sched_barrier(0);
      }
    }
    u32x4 va[16]; u32x2 vb[16];
#pragma unroll
    for (int i = 0; i < 8; ++i) {
      va[i] = __builtin_amdgcn_raw_buffer_load_b128(vrs, loA, i * 1536, 2);
      vb[i] = __builtin_amdgcn_raw_buffer_load_b64(vrs, loB, i * 1536, 2);
    }
    __builtin_amdgcn_sched_barrier(0);
    STAMP(1);
    if (waitq) { while (LDS_LD(&S.qflag) == 0u) __builtin_amdgcn_s_sleep(1); }
    asm volatile("" ::: "memory");
    STAMP(2);
    const u32x4 qa = *(const u32x4*)&S.qpk[4 * c16];
    const u32x2 qb = *(const u32x2*)&S.qpk[64 + 2 * c16];
    float d[16];
#pragma unroll
    for (int hh = 0; hh < 2; ++hh) {
#pragma unroll
      for (int i = hh * 8; i < hh * 8 + 8; ++i) {
        float t = dot2(ka[i][0], qa[0], 0.f);
        t = dot2(ka[i][1], qa[1], t); t = dot2(ka[i][2], qa[2], t); t = dot2(ka[i][3], qa[3], t);
        t = dot2(kb[i][0], qb[0], t); d[i] = dot2(kb[i][1], qb[1], t);
      }
      __builtin_amdgcn_sched_barrier(0);
#pragma unroll
      for (int i = 8 + hh * 4; i < 12 + hh * 4; ++i) {
        va[i] = __builtin_amdgcn_raw_buffer_load_b128(vrs, loA, i * 1536, 2);
        vb[i] = __builtin_amdgcn_raw_buffer_load_b64(vrs, loB, i * 1536, 2);
      }
      __builtin_amdgcn_sched_barrier(0);
    }
#define TSTEP(A, N, CTRL, BIT)                                                                 \
  _Pragma("unroll") for (int i = 0; i < N; ++i) {                                              \
    const bool hb = (c & BIT) != 0;                                                            \
    const float keep = hb ? A[i + N] : A[i], send = hb ? A[i] : A[i + N];                      \
    A[i] = keep + dpp<CTRL>(send);                                                             \
  }
    TSTEP(d, 8, XOR8, 8) TSTEP(d, 4, XOR7, 4) TSTEP(d, 2, XOR2, 2) TSTEP(d, 1, XOR1, 1)
    const float sc = c16 < ng ? rbf(rbf(d[0]) * 0.07216878364870322f) * 1.4426950408889634f : -INFINITY;
    const float mw = wave_max(sc);
    const float p = __builtin_amdgcn_exp2f(sc - mw);
    const float lw = wave_sum(p);
    S.pl[slotw][lane] = p;
    if (lane == 0) { S.ml[slotw][0] = mw; S.ml[slotw][1] = lw; }
    STAMP(3);
    asm volatile("s_waitcnt lgkmcnt(0)" ::: "memory");
    float acc[12];
#pragma unroll
    for (int k = 0; k < 12; ++k) acc[k] = 0.f;
#pragma unroll
    for (int i4 = 0; i4 < 4; ++i4) {
      const f32x4 pp = *(const f32x4*)&S.pl[slotw][r4 * 16 + i4 * 4];
#pragma unroll
      for (int j = 0; j < 4; ++j) {
        const u32x4 v = va[i4 * 4 + j]; const u32x2 w = vb[i4 * 4 + j];
#pragma unroll
        for (int k = 0; k < 4; ++k) {
          acc[2 * k] = fmaf(pp[j], lo2f(v[k]), acc[2 * k]);
          acc[2 * k + 1] = fmaf(pp[j], hi2f(v[k]), acc[2 * k + 1]);
        }
#pragma unroll
        for (int k = 0; k < 2; ++k) {
          acc[8 + 2 * k] = fmaf(pp[j], lo2f(w[k]), acc[8 + 2 * k]);
          acc[8 + 2 * k + 1] = fmaf(pp[j], hi2f(w[k]), acc[8 + 2 * k + 1]);
        }
      }
    }
#pragma unroll
    for (int k = 0; k < 6; ++k) acc[k] = sw32sum(acc[k], acc[k + 6]);
    float* dst = &S.red[slotw * 2 + (r4 & 1)][0];
    if (half == 0) {
      *(f32x4*)&dst[8 * c16] = f32x4{acc[0], acc[1], acc[2], acc[3]};
      *(f32x2*)&dst[8 * c16 + 4] = f32x2{acc[4], acc[5]};
    } else {
      *(f32x2*)&dst[8 * c16 + 6] = f32x2{acc[0], acc[1]};
      *(f32x4*)&dst[128 + 4 * c16] = f32x4{acc[2], acc[3], acc[4], acc[5]};
    }
    STAMP(4); STAMPT(448, 19); STAMPT(256, 20); STAMPT(704, 18);
  };

  if (wave == 11 && isO) {
    stream_body(8, 8, 512, true);
  } else if (wave >= 8) {
    const int st = tid - 512;
    if (isQ) {
      u32 xr[2][5];
#pragma unroll
      for (int bb = 0; bb < 2; ++bb) {
        const u32* xp = (const u32*)(P.x + bb * D) + lane * 5;
#pragma unroll
        for (int i = 0; i < 5; ++i) xr[bb][i] = xp[i];
      }
      const int s2 = st < 192 ? st : 191, cc = s2 % 24, slot = s2 / 24;
      u32x4 wqr[16];
      {
        const __amdgpu_buffer_rsrc_t rs = mkrsrc(P.wq + (size_t)(jr * 128) * QN + h * DH, 128 * QN * 2);
        const u32 vo = (u32)(slot * 16) * (QN * 2) + cc * 16;
#pragma unroll
        for (int i = 0; i < 16; ++i) wqr[i] = __builtin_amdgcn_raw_buffer_load_b128(rs, vo, i * QN * 2, 2);
      }
      {
        float xv[2][10], sm[2], sq[2];
#pragma unroll
        for (int bb = 0; bb < 2; ++bb) {
#pragma unroll
          for (int i = 0; i < 5; ++i) { xv[bb][2 * i] = lo2f(xr[bb][i]); xv[bb][2 * i + 1] = hi2f(xr[bb][i]); }
          sm[bb] = ((xv[bb][0] + xv[bb][1]) + (xv[bb][2] + xv[bb][3])) + ((xv[bb][4] + xv[bb][5]) + (xv[bb][6] + xv[bb][7])) + (xv[bb][8] + xv[bb][9]);
        }
        sm[0] = wave_sum(sm[0]) * (1.f / D); sm[1] = wave_sum(sm[1]) * (1.f / D);
#pragma unroll
        for (int bb = 0; bb < 2; ++bb) {
          float q0 = 0.f, q1 = 0.f;
#pragma unroll
          for (int i = 0; i < 5; ++i) { const float d0 = xv[bb][2 * i] - sm[bb], d1 = xv[bb][2 * i + 1] - sm[bb]; q0 = fmaf(d0, d0, q0); q1 = fmaf(d1, d1, q1); }
          sq[bb] = q0 + q1;
        }
        sq[0] = wave_sum(sq[0]) * (1.f / D); sq[1] = wave_sum(sq[1]) * (1.f / D);
#pragma unroll
        for (int bb = 0; bb < 2; ++bb) {
          const float mb = rbf(sm[bb]), rden = __builtin_amdgcn_rcpf(rbf(__builtin_amdgcn_sqrtf(rbf(rbf(sq[bb]) + 1e-5f))));
#pragma unroll
          for (int i = 0; i < 5; ++i) S.xnp[bb][lane * 5 + i] = pk2(rbf(xv[bb][2 * i] - mb) * rden, rbf(xv[bb][2 * i + 1] - mb) * rden);
        }
      }
      asm volatile("s_waitcnt lgkmcnt(0)" ::: "memory");
      float a[2][8];
#pragma unroll
      for (int k = 0; k < 8; ++k) { a[0][k] = 0.f; a[1][k] = 0.f; }
#pragma unroll
      for (int i4 = 0; i4 < 2; ++i4) {
        const u32x4 x0 = *(const u32x4*)&S.xnp[0][jr * 64 + slot * 8 + i4 * 4];
        const u32x4 x1 = *(const u32x4*)&S.xnp[1][jr * 64 + slot * 8 + i4 * 4];
#pragma unroll
        for (int i = 0; i < 4; ++i) {
          const u32x4 w0 = wqr[(i4 * 4 + i) * 2], w1 = wqr[(i4 * 4 + i) * 2 + 1];
#pragma unroll
          for (int k = 0; k < 4; ++k) {
            const u32 wl = plo(w0[k], w1[k]), wh = phi(w0[k], w1[k]);
            a[0][2 * k] = dot2(wl, x0[i], a[0][2 * k]); a[0][2 * k + 1] = dot2(wh, x0[i], a[0][2 * k + 1]);
            a[1][2 * k] = dot2(wl, x1[i], a[1][2 * k]); a[1][2 * k + 1] = dot2(wh, x1[i], a[1][2 * k + 1]);
          }
        }
      }
      if (st < 192) {
#pragma unroll
        for (int bb = 0; bb < 2; ++bb) {
          *(f32x4*)&S.qpart[slot][bb][cc * 8] = f32x4{a[bb][0], a[bb][1], a[bb][2], a[bb][3]};
          *(f32x4*)&S.qpart[slot][bb][cc * 8 + 4] = f32x4{a[bb][4], a[bb][5], a[bb][6], a[bb][7]};
        }
      }
      asm volatile("s_waitcnt lgkmcnt(0)" ::: "memory");
      if (lane == 0) __hip_atomic_fetch_add(&S.qcnt, 1u, __ATOMIC_RELAXED, __HIP_MEMORY_SCOPE_WORKGROUP);
      while (LDS_LD(&S.qcnt) != 3u) __builtin_amdgcn_s_sleep(1);
      asm volatile("" ::: "memory");
      STAMPT(512, 14);
      GET_TAGS();
      if (st < 192) {
        const int bb = st / 96, k = st % 96;
        float s0 = 0.f, s1 = 0.f;
#pragma unroll
        for (int w = 0; w < 8; ++w) { const f32x2 t = *(const f32x2*)&S.qpart[w][bb][2 * k]; s0 += t[0]; s1 += t[1]; }
        st_gran(P.ws + WS_Q + ((size_t)((h * 2 + bb) * 5 + jr) * 96 + k) * 16, s0, s1, ta, tb);
      }
    }
    if (wave == 8) {
      const unsigned char* pq = P.ws + WS_Q + (size_t)(h * 2 + b) * 480 * 16;
      const unsigned char* p0 = pq + lane * 16;
      const unsigned char* p1 = pq + (64 + c) * 16;
      u32x4 g[10];
      GET_TAGS();
      for (int it = 0; it < SPIN_LIMIT; ++it) {
        ld_gran10(p0, p0 + 4608, p1, p1 + 4608, g);
        bool ok = true;
#pragma unroll
        for (int i = 0; i < 10; ++i) ok = ok && TAGOK(g[i]);
        if (__all(ok)) break;
        __builtin_amdgcn_s_sleep(2);
      }
      STAMPT(512, 15);
      float s0 = 0.f, s1 = 0.f, t0 = 0.f, t1 = 0.f;
#pragma unroll
      for (int i = 0; i < 5; ++i) {
        s0 += __uint_as_float(g[i][0]); s1 += __uint_as_float(g[i][2]);
        t0 += __uint_as_float(g[5 + i][0]); t1 += __uint_as_float(g[5 + i][2]);
      }
      S.qpk[lane] = pk2(s0, s1);
      if (lane < 32) S.qpk[64 + lane] = pk2(t0, t1);
      asm volatile("s_waitcnt lgkmcnt(0)" ::: "memory");
      if (lane == 0) __hip_atomic_store(&S.qflag, 1u, __ATOMIC_RELAXED, __HIP_MEMORY_SCOPE_WORKGROUP);
    } else {
      __builtin_amdgcn_s_setprio(3);
      const int tw = st - 64;
      if (isO) {
        const __amdgpu_buffer_rsrc_t ro = mkrsrc(P.wo + (size_t)(h * DH + jr * 32) * D, 32 * D * 2);
        u32x4 w0[10], w1[10];
#pragma unroll
        for (int i = 0; i < 10; ++i) {
          const int task = tw + 128 * i, k = task / 80, ch = task % 80;
          w0[i] = __builtin_amdgcn_raw_buffer_load_b128(ro, (u32)(2 * k) * (D * 2) + ch * 16, 0, 2);
          w1[i] = __builtin_amdgcn_raw_buffer_load_b128(ro, (u32)(2 * k) * (D * 2) + ch * 16, D * 2, 2);
        }
#pragma unroll
        for (int i = 0; i < 10; ++i) {
          const int task = tw + 128 * i, k = task / 80, ch = task % 80;
          u32* d = &S.wst[k * 640 + ch * 8];
          *(u32x4*)d = u32x4{plo(w0[i][0], w1[i][0]), phi(w0[i][0], w1[i][0]), plo(w0[i][1], w1[i][1]), phi(w0[i][1], w1[i][1])};
          *(u32x4*)(d + 4) = u32x4{plo(w0[i][2], w1[i][2]), phi(w0[i][2], w1[i][2]), plo(w0[i][3], w1[i][3]), phi(w0[i][3], w1[i][3])};
        }
      } else {
#pragma unroll
        for (int m = 0; m < 2; ++m) {
          if ((m == 0 && GMODE != 2) || (m == 1 && GMODE == 1)) {
            const __amdgpu_buffer_rsrc_t rg = mkrsrc((m ? P.wu : P.wg) + fidx * 24, (D * F - fidx * 24) * 2);
            u32x4 a0[5], a1[5];
#pragma unroll
            for (int i = 0; i < 5; ++i) {
              const int task = tw + 192 * i, k = task / 3, q = task % 3;
              const u32 vo = (u32)(2 * k) * (F * 2) + q * 16;
              a0[i] = __builtin_amdgcn_raw_buffer_load_b128(rg, vo, 0, GUAUX);
              a1[i] = __builtin_amdgcn_raw_buffer_load_b128(rg, vo, F * 2, GUAUX);
            }
#pragma unroll
            for (int i = 0; i < 5; ++i) {
              const int task = tw + 192 * i, k = task / 3, q = task % 3;
#pragma unroll
              for (int e = 0; e < 4; ++e) {
                S.wst[(m * 24 + q * 8 + 2 * e) * GUS + k] = plo(a0[i][e], a1[i][e]);
                S.wst[(m * 24 + q * 8 + 2 * e + 1) * GUS + k] = phi(a0[i][e], a1[i][e]);
              }
            }
          }
        }
        const __amdgpu_buffer_rsrc_t rd = mkrsrc(P.wd + (size_t)(fidx * 24) * D, 24 * D * 2);
        u32x4 w0[5], w1[5];
#pragma unroll
        for (int i = 0; i < 5; ++i) {
          const int task = tw + 192 * i, k = task / 80, ch = task % 80;
          w0[i] = __builtin_amdgcn_raw_buffer_load_b128(rd, (u32)(2 * k) * (D * 2) + ch * 16, 0, 2);
          w1[i] = __builtin_amdgcn_raw_buffer_load_b128(rd, (u32)(2 * k) * (D * 2) + ch * 16, D * 2, 2);
        }
#pragma unroll
        for (int i = 0; i < 5; ++i) {
          const int task = tw + 192 * i, k = task / 80, ch = task % 80;
          u32* d = &S.wst[48 * GUS + k * 640 + ch * 8];
          *(u32x4*)d = u32x4{plo(w0[i][0], w1[i][0]), phi(w0[i][0], w1[i][0]), plo(w0[i][1], w1[i][1]), phi(w0[i][1], w1[i][1])};
          *(u32x4*)(d + 4) = u32x4{plo(w0[i][2], w1[i][2]), phi(w0[i][2], w1[i][2]), plo(w0[i][3], w1[i][3]), phi(w0[i][3], w1[i][3])};
        }
      }
      STAMPT(576, 16);
    }
  } else {
    stream_body(wave, isF ? 13 : 16, isF ? wave * 52 : wave * 64, true);
  }
  __syncthreads();
  GET_TAGS();
  float hres0 = 0.f, hres1 = 0.f;
  const int t3 = tid < 320 ? tid : 319;
  const int gslot = (tid >> 2) * 8 + (tid & 3);
  u32x2 wpre[16];
  if (wave < 2) {
    float mm[9];
    float Mwg = -INFINITY;
#pragma unroll
    for (int w = 0; w < 9; ++w) { mm[w] = (w < 8 || isO) ? S.ml[w][0] : -INFINITY; Mwg = fmaxf(Mwg, mm[w]); }
    float Lwg = 0.f;
#pragma unroll
    for (int w = 0; w < 9; ++w) { mm[w] = __builtin_amdgcn_exp2f(mm[w] - Mwg); Lwg = fmaf(mm[w], (w < 8 || isO) ? S.ml[w][1] : 0.f, Lwg); }
    const int t = tid < 97 ? (tid > 0 ? tid - 1 : 0) : 95;
    float o0 = 0.f, o1 = 0.f;
#pragma unroll
    for (int w = 0; w < 9; ++w) {
      if (w < 8 || isO) {
        const f32x2 a = *(const f32x2*)&S.red[2 * w][2 * t], bb = *(const f32x2*)&S.red[2 * w + 1][2 * t];
        o0 = fmaf(mm[w], a[0] + bb[0], o0); o1 = fmaf(mm[w], a[1] + bb[1], o1);
      }
    }
    if (tid == 0) { o0 = Mwg; o1 = Lwg; }
    if (tid < 97) {
      st_gran(P.ws + WS_O + ((size_t)prod * 128 + tid) * 16, o0, o1, ta, tb);
      *(f32x2*)&S.oown[tid * 2] = f32x2{o0, o1};
    }
    asm volatile("s_waitcnt lgkmcnt(0)" ::: "memory");
    if (lane == 0) __hip_atomic_fetch_add(&S.ocnt, 1u, __ATOMIC_RELAXED, __HIP_MEMORY_SCOPE_WORKGROUP);
    STAMP(5);
  } else if (isO && wave < 4) {
    const int bb = wave - 2, pj = bb * 4 + r4;
    const bool own = pj == j8;
    const unsigned char* pb = P.ws + WS_O + (size_t)(h * 8 + pj) * 128 * 16;
    u32x4 go, gm;
    for (int it = 0; it < SPIN_LIMIT; ++it) {
      ld_gran2(pb + (1 + jr * 16 + c16) * 16, pb, go, gm);
      if (__all(own || (TAGOK(go) && TAGOK(gm)))) break;
    }
    while (LDS_LD(&S.ocnt) != 2u) __builtin_amdgcn_s_sleep(1);
    asm volatile("" ::: "memory");
    const f32x2 om = *(const f32x2*)&S.oown[0], ov = *(const f32x2*)&S.oown[(1 + jr * 16 + c16) * 2];
    const float m = own ? om[0] : __uint_as_float(gm[0]), l = own ? om[1] : __uint_as_float(gm[2]);
    const float v0 = own ? ov[0] : __uint_as_float(go[0]), v1 = own ? ov[1] : __uint_as_float(go[2]);
    const float M = x1632max(m);
    const float e = __builtin_amdgcn_exp2f(m - M);
    const float L = x1632sum(e * l);
    const float a0 = x1632sum(e * v0), a1 = x1632sum(e * v1);
    const float rl = __builtin_amdgcn_rcpf(L);
    if (lane < 16) S.opk[bb][lane] = pk2(a0 * rl, a1 * rl);
    asm volatile("s_waitcnt lgkmcnt(0)" ::: "memory");
    if (lane == 0) __hip_atomic_fetch_add(&S.oflag, 1u, __ATOMIC_RELAXED, __HIP_MEMORY_SCOPE_WORKGROUP);
    STAMPT(128, 6);
  } else if (isO && wave >= 7) {
    const int yt = tid - 448;
#pragma unroll
    for (int k = 0; k < 16; ++k) wpre[k] = *(const u32x2*)&S.wst[k * 640 + 2 * yt];
    while (LDS_LD(&S.oflag) != 2u) __builtin_amdgcn_s_sleep(1);
    asm volatile("" ::: "memory");
    float y00 = 0.f, y01 = 0.f, y10 = 0.f, y11 = 0.f;
#pragma unroll
    for (int k4 = 0; k4 < 4; ++k4) {
      const u32x4 oa = *(const u32x4*)&S.opk[0][k4 * 4], ob = *(const u32x4*)&S.opk[1][k4 * 4];
#pragma unroll
      for (int k = 0; k < 4; ++k) {
        const u32x2 w = wpre[k4 * 4 + k];
        y00 = dot2(w[0], oa[k], y00); y01 = dot2(w[1], oa[k], y01);
        y10 = dot2(w[0], ob[k], y10); y11 = dot2(w[1], ob[k], y11);
      }
    }
    unsigned char* py = P.ws + WS_Y + ((size_t)(h * 6 + jr) * 640 + (yt >> 2) * 8 + (yt & 3)) * 16;
    st_gran(py, y00, y01, ta, tb);
    st_gran(py + 64, y10, y11, ta, tb);
    if (lane == 0) __hip_atomic_fetch_add(&S.ycnt, 1u, __ATOMIC_RELAXED, __HIP_MEMORY_SCOPE_WORKGROUP);
    STAMPT(448, 7);
  }
  if (isR && wave < 6) {
    const unsigned char* p0 = P.ws + WS_Y + ((size_t)(tid >> 3) * 640 + rid * 8 + (tid & 7)) * 16;
    constexpr size_t ST = (size_t)48 * 640 * 16;
    u32x4 ga, gb, gc, gd;
    while (LDS_LD(&S.ycnt) != 5u) __builtin_amdgcn_s_sleep(2);
    for (int it = 0; it < SPIN_LIMIT; ++it) {
      ld_gran4(p0, p0 + ST, p0 + 2 * ST, p0 + 3 * ST, ga, gb, gc, gd);
      if (__all(TAGOK(ga) && TAGOK(gb) && TAGOK(gc) && TAGOK(gd))) break;
      __builtin_amdgcn_s_sleep(YSLEEP);
    }
    STAMP(8);
    float y0 = (__uint_as_float(ga[0]) + __uint_as_float(gb[0])) + (__uint_as_float(gc[0]) + __uint_as_float(gd[0]));
    float y1 = (__uint_as_float(ga[2]) + __uint_as_float(gb[2])) + (__uint_as_float(gc[2]) + __uint_as_float(gd[2]));
    y0 += dpp<XOR8>(y0); y1 += dpp<XOR8>(y1);
    y0 = x1632sum(y0); y1 = x1632sum(y1);
    if (lane < 8) *(f32x2*)&S.wsum[wave][lane * 2] = f32x2{y0, y1};
    asm volatile("s_waitcnt lgkmcnt(0)" ::: "memory");
    if (lane == 0) __hip_atomic_fetch_add(&S.hcnt, 1u, __ATOMIC_RELAXED, __HIP_MEMORY_SCOPE_WORKGROUP);
    if (wave == 0) {
      while (LDS_LD(&S.hcnt) != 6u) __builtin_amdgcn_s_sleep(1);
      asm volatile("" ::: "memory");
      if (tid < 8) {
        float a0 = 0.f, a1 = 0.f;
#pragma unroll
        for (int w = 0; w < 6; ++w) { a0 += S.wsum[w][tid * 2]; a1 += S.wsum[w][tid * 2 + 1]; }
        hres0 = rbf(lo2f(xres) + rbf(a0)); hres1 = rbf(hi2f(xres) + rbf(a1));
        st_gran(P.ws + WS_H + (size_t)(rid * 8 + tid) * 16, hres0, hres1, ta, tb);
      }
      if (lane == 0) __hip_atomic_store(&S.hflag, 1u, __ATOMIC_RELAXED, __HIP_MEMORY_SCOPE_WORKGROUP);
      STAMP(9);
    }
  }
  if (isF) {
    if (wave >= 8) {
      while (LDS_LD(&S.ocnt) != 2u) __builtin_amdgcn_s_sleep(1);
      asm volatile("" ::: "memory");
      const int st = tid - 512;
#pragma unroll
      for (int m = 0; m < 2; ++m) {
        if ((m == 0 && GMODE == 2) || (m == 1 && GMODE != 1)) {
          const __amdgpu_buffer_rsrc_t ru = mkrsrc((m ? P.wu : P.wg) + fidx * 24, (D * F - fidx * 24) * 2);
          u32x4 a0[4], a1[4];
#pragma unroll
          for (int i = 0; i < 4; ++i) {
            const int task = st + 256 * i, k = task / 3, q = task % 3;
            const u32 vo = task < 960 ? (u32)(2 * k) * (F * 2) + q * 16 : 0x7fffff00u;
            a0[i] = __builtin_amdgcn_raw_buffer_load_b128(ru, vo, 0, GUAUX);
            a1[i] = __builtin_amdgcn_raw_buffer_load_b128(ru, vo, F * 2, GUAUX);
          }
#pragma unroll
          for (int i = 0; i < 4; ++i) {
            const int task = st + 256 * i, k = task / 3, q = task % 3;
            if (task < 960) {
#pragma unroll
              for (int e = 0; e < 4; ++e) {
                S.wst[(m * 24 + q * 8 + 2 * e) * GUS + k] = plo(a0[i][e], a1[i][e]);
                S.wst[(m * 24 + q * 8 + 2 * e + 1) * GUS + k] = phi(a0[i][e], a1[i][e]);
              }
            }
          }
        }
      }
      STAMPT(512, 17);
    }
    __syncthreads();
    u32x4 wreg[5];
    {
      const u32* wrow = &S.wst[((r4 >> 1) * 24 + 2 * wave + (r4 & 1)) * GUS + 20 * c16];
#pragma unroll
      for (int i = 0; i < 5; ++i) wreg[i] = *(const u32x4*)&wrow[4 * i];
#pragma unroll
      for (int k = 0; k < 12; ++k) wpre[k] = *(const u32x2*)&S.wst[48 * GUS + k * 640 + 2 * t3];
    }
    float cs = 0.f;
#pragma unroll
    for (int i = 0; i < 5; ++i)
#pragma unroll
      for (int k = 0; k < 4; ++k) cs = dot2(wreg[i][k], 0x3f803f80u, cs);
    cs = row16sum(cs);
    if (tid < 320) {
      const unsigned char* p0 = P.ws + WS_H + (size_t)gslot * 16;
      u32x4 ga, gb;
      for (int it = 0; it < SPIN_LIMIT; ++it) {
        ld_gran2(p0, p0 + 64, ga, gb);
        if (__all(TAGOK(ga) && TAGOK(gb))) break;
      }
      STAMP(10);
      const float h00 = __uint_as_float(ga[0]), h01 = __uint_as_float(ga[2]), h10 = __uint_as_float(gb[0]), h11 = __uint_as_float(gb[2]);
      S.hp[0][tid] = (ga[0] >> 16) | (ga[2] & 0xffff0000u);
      S.hp[1][tid] = (gb[0] >> 16) | (gb[2] & 0xffff0000u);
      float s0 = h00 + h01, q0 = h00 * h00 + h01 * h01, s1 = h10 + h11, q1 = h10 * h10 + h11 * h11;
      s0 = sw32sum(s0, s1); q0 = sw32sum(q0, q1);
      s0 = sw16sum(s0, q0);
      s0 = row16sum(s0);
      if (c16 == 0) S.st[wave][r4] = s0;
    }
    __syncthreads();
    {
      const int bsel = c16 & 1;
      float d0 = 0.f, d1 = 0.f;
#pragma unroll
      for (int i = 0; i < 5; ++i) {
        const u32x4 ha = *(const u32x4*)&S.hp[0][20 * c16 + 4 * i], hb = *(const u32x4*)&S.hp[1][20 * c16 + 4 * i];
#pragma unroll
        for (int k = 0; k < 4; ++k) { d0 = dot2(wreg[i][k], ha[k], d0); d1 = dot2(wreg[i][k], hb[k], d1); }
      }
      float sm = 0.f, sq = 0.f;
#pragma unroll
      for (int w = 0; w < 5; ++w) { const f32x2 t = *(const f32x2*)&S.st[w][bsel * 2]; sm += t[0]; sq += t[1]; }
      const float mean = sm * (1.f / D);
      const float var = fmaxf(sq * (1.f / D) - mean * mean, 0.f);
      const float mb = rbf(mean), den = rbf(__builtin_amdgcn_sqrtf(rbf(rbf(var) + 1e-5f)));
      const float rden = __builtin_amdgcn_rcpf(den);
      float dd = (bsel ? d1 : d0) + dpp<XOR1>(bsel ? d0 : d1);
      dd += dpp<XOR2>(dd); dd += dpp<XOR1>(dpp<XOR7>(dd)); dd += dpp<XOR8>(dd);
      const float mine = rbf((dd - mb * cs) * rden);
      const float up = upper32(mine);
      const float t = 0.7978845608028654f * (mine + 0.044715f * mine * mine * mine);
      const float th = 1.f - 2.f * __builtin_amdgcn_rcpf(__builtin_amdgcn_exp2f(t * 2.8853900817779268f) + 1.f);
      const float gval = rbf(rbf(0.5f * mine * (1.f + th)) * up);
      if (half == 0 && c16 < 2) ((uint16_t*)&S.gp[c16][0])[2 * wave + r4] = (uint16_t)(__float_as_uint(gval) >> 16);
    }
    __syncthreads();
    if (tid < 320) {
      float z00 = 0.f, z01 = 0.f, z10 = 0.f, z11 = 0.f;
#pragma unroll
      for (int k4 = 0; k4 < 3; ++k4) {
        const u32x4 oa = *(const u32x4*)&S.gp[0][k4 * 4], ob = *(const u32x4*)&S.gp[1][k4 * 4];
#pragma unroll
        for (int k = 0; k < 4; ++k) {
          const u32x2 w = wpre[k4 * 4 + k];
          z00 = dot2(w[0], oa[k], z00); z01 = dot2(w[1], oa[k], z01);
          z10 = dot2(w[0], ob[k], z10); z11 = dot2(w[1], ob[k], z11);
        }
      }
      unsigned char* pz = P.ws + WS_Z + ((size_t)fidx * 640 + gslot) * 16;
      st_gran(pz, z00, z01, ta, tb);
      st_gran(pz + 64, z10, z11, ta, tb);
    }
    STAMP(11);
  }
  if (isR && wave < 9) {
    if (wave > 0) {
      const int zt = tid - 64;
      const unsigned char* p0 = P.ws + WS_Z + ((size_t)(zt >> 3) * 640 + rid * 8 + (zt & 7)) * 16;
      u32x4 ga;
      while (LDS_LD(&S.hflag) == 0u) __builtin_amdgcn_s_sleep(2);
      for (int it = 0; it < SPIN_LIMIT; ++it) {
        ld_gran1(p0, ga);
        if (__all(TAGOK(ga))) break;
        __builtin_amdgcn_s_sleep(YSLEEP);
      }
      STAMPT(64, 12);
      float y0 = __uint_as_float(ga[0]), y1 = __uint_as_float(ga[2]);
      y0 += dpp<XOR8>(y0); y1 += dpp<XOR8>(y1);
      y0 = x1632sum(y0); y1 = x1632sum(y1);
      if (lane < 8) *(f32x2*)&S.zsum[wave - 1][lane * 2] = f32x2{y0, y1};
      asm volatile("s_waitcnt lgkmcnt(0)" ::: "memory");
      if (lane == 0) __hip_atomic_fetch_add(&S.zcnt, 1u, __ATOMIC_RELAXED, __HIP_MEMORY_SCOPE_WORKGROUP);
    } else {
      while (LDS_LD(&S.zcnt) != 8u) __builtin_amdgcn_s_sleep(1);
      asm volatile("" ::: "memory");
      if (tid < 8) {
        float a0 = 0.f, a1 = 0.f;
#pragma unroll
        for (int w = 0; w < 8; ++w) { a0 += S.zsum[w][tid * 2]; a1 += S.zsum[w][tid * 2 + 1]; }
        const u32 o = pk2(hres0 + rbf(a0), hres1 + rbf(a1));
        ((u32*)(P.out + (tid >> 2) * D + rid * 8))[tid & 3] = o;
      }
      if (wg == 0 && tid == 0) {
        u32x2 ne = {ta, tb};
        asm volatile("global_store_dwordx2 %0, %1, off sc1" :: "v"(P.ws + WS_EPOCH), "v"(ne) : "memory");
      }
      STAMP(13);
    }
  }
}
}

extern "C" void kernel_launch(void* const* d_in, const int* in_sizes, int n_in, void* d_out, int out_size,
                              void* d_ws, size_t ws_size, hipStream_t stream) {
  Params P;
  P.x = (const uint16_t*)d_in[0]; P.kc = (const uint16_t*)d_in[1]; P.vc = (const uint16_t*)d_in[2];
  P.wq = (const uint16_t*)d_in[3]; P.wo = (const uint16_t*)d_in[4];
  P.wg = (const uint16_t*)d_in[6]; P.wu = (const uint16_t*)d_in[7]; P.wd = (const uint16_t*)d_in[8];
  P.out = (uint16_t*)d_out; P.ws = (unsigned char*)d_ws;
  fused_kernel<<<NWG, NT, 0, stream>>>(P);
}
```

```cpp
#include <hip/hip_runtime.h>
#include <hip/hip_bf16.h>
#include <stdint.h>

namespace {
typedef unsigned u32;
typedef u32 u32x2 __attribute__((ext_vector_type(2)));
typedef u32 u32x4 __attribute__((ext_vector_type(4)));
typedef float f32x2 __attribute__((ext_vector_type(2)));
typedef float f32x4 __attribute__((ext_vector_type(4)));
typedef __bf16 bf16x2 __attribute__((ext_vector_type(2)));

constexpr int D = 640, H = 32, SK = 8192, DH = 192, W = 2048, F = 1536, QN = H * DH;
constexpr int NWG = 256, NT = 768, NRED = 80;
constexpr int SPIN_LIMIT = 1 << 20;

constexpr size_t WS_EPOCH = 0;
constexpr size_t WS_Q = 4096;
constexpr size_t WS_O = WS_Q + (size_t)32 * 2 * 480 * 16;
constexpr size_t WS_H = WS_O + (size_t)256 * 128 * 16;
constexpr size_t WS_Y = WS_H + 640 * 16;
constexpr size_t WS_Z = WS_Y + (size_t)192 * 640 * 16;
constexpr size_t WS_END = WS_Z + (size_t)64 * 640 * 16;

__device__ __forceinline__ float rbf(float f) { return __bfloat162float(__float2bfloat16(f)); }
__device__ __forceinline__ float lo2f(u32 v) { return __uint_as_float(v << 16); }
__device__ __forceinline__ float hi2f(u32 v) { return __uint_as_float(v & 0xffff0000u); }
__device__ __forceinline__ u32 pk2(float lo, float hi) {
  u32 r; asm("v_cvt_pk_bf16_f32 %0, %1, %2" : "=v"(r) : "v"(lo), "v"(hi)); return r;
}
__device__ __forceinline__ float dot2(u32 a, u32 b, float c) {
  return __builtin_amdgcn_fdot2_f32_bf16(__builtin_bit_cast(bf16x2, a), __builtin_bit_cast(bf16x2, b), c, false);
}
__device__ __forceinline__ u32 plo(u32 w0, u32 w1) { return __builtin_amdgcn_perm(w1, w0, 0x05040100); }
__device__ __forceinline__ u32 phi(u32 w0, u32 w1) { return __builtin_amdgcn_perm(w1, w0, 0x07060302); }
template <int CTRL> __device__ __forceinline__ float dpp(float x) {
  return __builtin_bit_cast(float, __builtin_amdgcn_mov_dpp(__builtin_bit_cast(int, x), CTRL, 0xf, 0xf, true));
}
constexpr int XOR1 = 0xB1, XOR2 = 0x4E, XOR7 = 0x141, XOR8 = 0x128;
__device__ __forceinline__ float sw16sum(float a, float b) {
  auto s = __builtin_amdgcn_permlane16_swap(__float_as_uint(a), __float_as_uint(b), false, false);
  return __uint_as_float(s[0]) + __uint_as_float(s[1]);
}
__device__ __forceinline__ float sw32sum(float a, float b) {
  auto s = __builtin_amdgcn_permlane32_swap(__float_as_uint(a), __float_as_uint(b), false, false);
  return __uint_as_float(s[0]) + __uint_as_float(s[1]);
}
__device__ __forceinline__ float upper32(float a) {
  auto s = __builtin_amdgcn_permlane32_swap(__float_as_uint(a), __float_as_uint(a), false, false);
  return __uint_as_float(s[1]);
}
__device__ __forceinline__ float x1632max(float x) {
  auto s = __builtin_amdgcn_permlane16_swap(__float_as_uint(x), __float_as_uint(x), false, false);
  x = fmaxf(__uint_as_float(s[0]), __uint_as_float(s[1]));
  auto t = __builtin_amdgcn_permlane32_swap(__float_as_uint(x), __float_as_uint(x), false, false);
  return fmaxf(__uint_as_float(t[0]), __uint_as_float(t[1]));
}
__device__ __forceinline__ float x1632sum(float x) { x = sw16sum(x, x); return sw32sum(x, x); }
__device__ __forceinline__ float row16sum(float x) {
  x += dpp<XOR1>(x); x += dpp<XOR2>(x); x += dpp<XOR7>(x); x += dpp<XOR8>(x); return x;
}
__device__ __forceinline__ float wave_sum(float x) { return x1632sum(row16sum(x)); }
__device__ __forceinline__ float wave_max(float x) {
  x = fmaxf(x, dpp<XOR1>(x)); x = fmaxf(x, dpp<XOR2>(x)); x = fmaxf(x, dpp<XOR7>(x)); x = fmaxf(x, dpp<XOR8>(x));
  return x1632max(x);
}
__device__ __forceinline__ void st_gran(unsigned char* p, float v0, float v1, u32 ta, u32 tb) {
  u32x4 g = {__float_as_uint(v0), ta, __float_as_uint(v1), tb};
  asm volatile("global_store_dwordx4 %0, %1, off sc1\n\ts_nop 1" :: "v"(p), "v"(g) : "memory");
}
__device__ __forceinline__ void ld_gran1(const unsigned char* p, u32x4& a) {
  asm volatile("global_load_dwordx4 %0, %1, off sc1\n\ts_waitcnt vmcnt(0)" : "=&v"(a) : "v"(p) : "memory");
}
__device__ __forceinline__ void ld_gran2(const unsigned char* p, const unsigned char* q, u32x4& a, u32x4& b) {
  asm volatile("global_load_dwordx4 %0, %2, off sc1\n\tglobal_load_dwordx4 %1, %3, off sc1\n\ts_waitcnt vmcnt(0)"
               : "=&v"(a), "=&v"(b) : "v"(p), "v"(q) : "memory");
}
__device__ __forceinline__ void ld_gran3(const unsigned char* p0, const unsigned char* p1, const unsigned char* p2,
                                         u32x4& a, u32x4& b, u32x4& c) {
  asm volatile("global_load_dwordx4 %0, %3, off sc1\n\tglobal_load_dwordx4 %1, %4, off sc1\n\t"
               "global_load_dwordx4 %2, %5, off sc1\n\ts_waitcnt vmcnt(0)"
               : "=&v"(a), "=&v"(b), "=&v"(c) : "v"(p0), "v"(p1), "v"(p2) : "memory");
}
__device__ __forceinline__ void ld_gran4(const unsigned char* p0, const unsigned char* p1, const unsigned char* p2,
                                         const unsigned char* p3, u32x4& a, u32x4& b, u32x4& c, u32x4& d) {
  asm volatile("global_load_dwordx4 %0, %4, off sc1\n\tglobal_load_dwordx4 %1, %5, off sc1\n\t"
               "global_load_dwordx4 %2, %6, off sc1\n\tglobal_load_dwordx4 %3, %7, off sc1\n\ts_waitcnt vmcnt(0)"
               : "=&v"(a), "=&v"(b), "=&v"(c), "=&v"(d) : "v"(p0), "v"(p1), "v"(p2), "v"(p3) : "memory");
}
__device__ __forceinline__ void ld_gran10(const unsigned char* p0, const unsigned char* p0b, const unsigned char* p1,
                                          const unsigned char* p1b, u32x4 (&g)[10]) {
  asm volatile(
      "global_load_dwordx4 %0, %10, off sc1\n\tglobal_load_dwordx4 %1, %10, off offset:1536 sc1\n\t"
      "global_load_dwordx4 %2, %10, off offset:3072 sc1\n\tglobal_load_dwordx4 %3, %11, off sc1\n\t"
      "global_load_dwordx4 %4, %11, off offset:1536 sc1\n\t"
      "global_load_dwordx4 %5, %12, off sc1\n\tglobal_load_dwordx4 %6, %12, off offset:1536 sc1\n\t"
      "global_load_dwordx4 %7, %12, off offset:3072 sc1\n\tglobal_load_dwordx4 %8, %13, off sc1\n\t"
      "global_load_dwordx4 %9, %13, off offset:1536 sc1\n\ts_waitcnt vmcnt(0)"
      : "=&v"(g[0]), "=&v"(g[1]), "=&v"(g[2]), "=&v"(g[3]), "=&v"(g[4]), "=&v"(g[5]), "=&v"(g[6]), "=&v"(g[7]),
        "=&v"(g[8]), "=&v"(g[9])
      : "v"(p0), "v"(p0b), "v"(p1), "v"(p1b) : "memory");
}
__device__ __forceinline__ __amdgpu_buffer_rsrc_t mkrsrc(const void* p, u32 bytes) {
  return __builtin_amdgcn_make_buffer_rsrc(const_cast<void*>(p), 0, bytes, 0x00020000);
}
#define TAGOK(g) ((g)[1] == ta && (g)[3] == tb)
#define LDS_LD(p) __hip_atomic_load((p), __ATOMIC_RELAXED, __HIP_MEMORY_SCOPE_WORKGROUP)

struct Params {
  const uint16_t* x; const uint16_t* kc; const uint16_t* vc; const uint16_t* wq; const uint16_t* wo;
  const uint16_t* wg; const uint16_t* wu; const uint16_t* wd; uint16_t* out; unsigned char* ws;
};

constexpr int GUS = 324;
struct Lds {
  u32 wst[48 * GUS + 12 * 640];
  u32 xnp[2][320];
  float qpart[8][2][192];
  u32 qpk[96];
  float pl[9][64];
  float ml[10][2];
  float red[18][192];
  u32 opk[2][16];
  float wsum[8][16];
  float st[5][4];
  float hres[16];
  u32 hp[2][320];
  u32 gp[2][12];
  float oown[200];
  float zsum[8][16];
  u32 qcnt, qflag, ocnt, oflag, hcnt, zcnt, ycnt, hflag;
};

__global__ void __launch_bounds__(NT, 3) fused_kernel(Params P) {
  __shared__ __attribute__((aligned(16))) Lds S;
  const int tid = threadIdx.x, lane = tid & 63, wave = __builtin_amdgcn_readfirstlane(tid >> 6);
  const int half = lane >> 5, c = lane & 31;
  const int r4 = lane >> 4, c16 = lane & 15;
  const int wg = blockIdx.x, xcd = wg & 7, idx = wg >> 3;
  const int h = (idx >> 3) * 8 + xcd, j8 = idx & 7, b = j8 >> 2, sp = j8 & 3;
  const int prod = h * 8 + j8;
  const bool isF = sp == 3, isO = !isF;
  const int jr = j8 - b;
  const bool isQ = isO && jr < 5;
  const int fidx = xcd * 8 + (idx >> 3) * 2 + b;
  const int rid = h * 5 + jr;
  const bool isR = isQ && rid < NRED;

  if (tid == 0) { S.qcnt = 0u; S.qflag = 0u; S.ocnt = 0u; S.oflag = 0u; S.hcnt = 0u; S.zcnt = 0u; S.ycnt = 0u; S.hflag = 0u; }
  __syncthreads();
  if (wave >= 8) __builtin_amdgcn_s_setprio(3);
  unsigned long long ep = __hip_atomic_load((const unsigned long long*)(P.ws + WS_EPOCH), __ATOMIC_RELAXED, __HIP_MEMORY_SCOPE_AGENT);
  u32 ta, tb;
#define GET_TAGS() do { asm volatile("" : "+v"(ep)); ta = (u32)ep + 1u; tb = (u32)(ep >> 32) + 1u; } while (0)
  const u32 xres = ((const u32*)(P.x + ((tid >> 2) & 1) * D + (rid % NRED) * 8))[tid & 3];
  auto stream_body = [&](const int slotw, const int ng, const int row0, const bool waitq) {
    const size_t kvoff = ((size_t)(b * H + h) * SK + (SK - W) + sp * 512 + row0) * DH;
    const __amdgpu_buffer_rsrc_t krs = mkrsrc(P.kc + kvoff, ng * 1536);
    const __amdgpu_buffer_rsrc_t vrs = mkrsrc(P.vc + kvoff, ng * 1536);
    const u32 loA = r4 * 384 + c16 * 16, loB = r4 * 384 + 256 + c16 * 8;
    u32x4 ka[16]; u32x2 kb[16];
#pragma unroll
    for (int i = 0; i < 16; ++i) {
      ka[i] = __builtin_amdgcn_raw_buffer_load_b128(krs, loA, i * 1536, 2);
      kb[i] = __builtin_amdgcn_raw_buffer_load_b64(krs, loB, i * 1536, 2);
    }
    u32x4 va[16]; u32x2 vb[16];
#pragma unroll
    for (int i = 0; i < 8; ++i) {
      va[i] = __builtin_amdgcn_raw_buffer_load_b128(vrs, loA, i * 1536, 2);
      vb[i] = __builtin_amdgcn_raw_buffer_load_b64(vrs, loB, i * 1536, 2);
    }
    __builtin_amdgcn_sched_barrier(0);
    if (waitq) { while (LDS_LD(&S.qflag) == 0u) __builtin_amdgcn_s_sleep(1); }
    asm volatile("" ::: "memory");
    const u32x4 qa = *(const u32x4*)&S.qpk[4 * c16];
    const u32x2 qb = *(const u32x2*)&S.qpk[64 + 2 * c16];
    float d[16];
#pragma unroll
    for (int hh = 0; hh < 2; ++hh) {
#pragma unroll
      for (int i = hh * 8; i < hh * 8 + 8; ++i) {
        float t = dot2(ka[i][0], qa[0], 0.f);
        t = dot2(ka[i][1], qa[1], t); t = dot2(ka[i][2], qa[2], t); t = dot2(ka[i][3], qa[3], t);
        t = dot2(kb[i][0], qb[0], t); d[i] = dot2(kb[i][1], qb[1], t);
      }
      __builtin_amdgcn_sched_barrier(0);
#pragma unroll
      for (int i = 8 + hh * 4; i < 12 + hh * 4; ++i) {
        va[i] = __builtin_amdgcn_raw_buffer_load_b128(vrs, loA, i * 1536, 2);
        vb[i] = __builtin_amdgcn_raw_buffer_load_b64(vrs, loB, i * 1536, 2);
      }
      __builtin_amdgcn_sched_barrier(0);
    }
#define TSTEP(A, N, CTRL, BIT)                                                                 \
  _Pragma("unroll") for (int i = 0; i < N; ++i) {                                              \
    const bool hb = (c & BIT) != 0;                                                            \
    const float keep = hb ? A[i + N] : A[i], send = hb ? A[i] : A[i + N];                      \
    A[i] = keep + dpp<CTRL>(send);                                                             \
  }
    TSTEP(d, 8, XOR8, 8) TSTEP(d, 4, XOR7, 4) TSTEP(d, 2, XOR2, 2) TSTEP(d, 1, XOR1, 1)
    const float sc = c16 < ng ? rbf(rbf(d[0]) * 0.07216878364870322f) * 1.4426950408889634f : -INFINITY;
    const float mw = wave_max(sc);
    const float p = __builtin_amdgcn_exp2f(sc - mw);
    const float lw = wave_sum(p);
    S.pl[slotw][lane] = p;
    if (lane == 0) { S.ml[slotw][0] = mw; S.ml[slotw][1] = lw; }
    asm volatile("s_waitcnt lgkmcnt(0)" ::: "memory");
    float acc[12];
#pragma unroll
    for (int k = 0; k < 12; ++k) acc[k] = 0.f;
#pragma unroll
    for (int i4 = 0; i4 < 4; ++i4) {
      const f32x4 pp = *(const f32x4*)&S.pl[slotw][r4 * 16 + i4 * 4];
#pragma unroll
      for (int j = 0; j < 4; ++j) {
        const u32x4 v = va[i4 * 4 + j]; const u32x2 w = vb[i4 * 4 + j];
#pragma unroll
        for (int k = 0; k < 4; ++k) {
          acc[2 * k] = fmaf(pp[j], lo2f(v[k]), acc[2 * k]);
          acc[2 * k + 1] = fmaf(pp[j], hi2f(v[k]), acc[2 * k + 1]);
        }
#pragma unroll
        for (int k = 0; k < 2; ++k) {
          acc[8 + 2 * k] = fmaf(pp[j], lo2f(w[k]), acc[8 + 2 * k]);
          acc[8 + 2 * k + 1] = fmaf(pp[j], hi2f(w[k]), acc[8 + 2 * k + 1]);
        }
      }
    }
#pragma unroll
    for (int k = 0; k < 6; ++k) acc[k] = sw32sum(acc[k], acc[k + 6]);
    float* dst = &S.red[slotw * 2 + (r4 & 1)][0];
    if (half == 0) {
      *(f32x4*)&dst[8 * c16] = f32x4{acc[0], acc[1], acc[2], acc[3]};
      *(f32x2*)&dst[8 * c16 + 4] = f32x2{acc[4], acc[5]};
    } else {
      *(f32x2*)&dst[8 * c16 + 6] = f32x2{acc[0], acc[1]};
      *(f32x4*)&dst[128 + 4 * c16] = f32x4{acc[2], acc[3], acc[4], acc[5]};
    }
  };

  if (wave >= 8) {
    const int st = tid - 512;
    if (isQ) {
      u32 xr[2][5];
#pragma unroll
      for (int bb = 0; bb < 2; ++bb) {
        const u32* xp = (const u32*)(P.x + bb * D) + lane * 5;
#pragma unroll
        for (int i = 0; i < 5; ++i) xr[bb][i] = xp[i];
      }
      const int s2 = st < 192 ? st : 191, cc = s2 % 24, slot = s2 / 24;
      u32x4 wqr[16];
      {
        const __amdgpu_buffer_rsrc_t rs = mkrsrc(P.wq + (size_t)(jr * 128) * QN + h * DH, 128 * QN * 2);
        const u32 vo = (u32)(slot * 16) * (QN * 2) + cc * 16;
#pragma unroll
        for (int i = 0; i < 16; ++i) wqr[i] = __builtin_amdgcn_raw_buffer_load_b128(rs, vo, i * QN * 2, 2);
      }
      {
        float xv[2][10], sm[2], sq[2];
#pragma unroll
        for (int bb = 0; bb < 2; ++bb) {
#pragma unroll
          for (int i = 0; i < 5; ++i) { xv[bb][2 * i] = lo2f(xr[bb][i]); xv[bb][2 * i + 1] = hi2f(xr[bb][i]); }
          sm[bb] = ((xv[bb][0] + xv[bb][1]) + (xv[bb][2] + xv[bb][3])) + ((xv[bb][4] + xv[bb][5]) + (xv[bb][6] + xv[bb][7])) + (xv[bb][8] + xv[bb][9]);
        }
        sm[0] = wave_sum(sm[0]) * (1.f / D); sm[1] = wave_sum(sm[1]) * (1.f / D);
#pragma unroll
        for (int bb = 0; bb < 2; ++bb) {
          float q0 = 0.f, q1 = 0.f;
#pragma unroll
          for (int i = 0; i < 5; ++i) { const float d0 = xv[bb][2 * i] - sm[bb], d1 = xv[bb][2 * i + 1] - sm[bb]; q0 = fmaf(d0, d0, q0); q1 = fmaf(d1, d1, q1); }
          sq[bb] = q0 + q1;
        }
        sq[0] = wave_sum(sq[0]) * (1.f / D); sq[1] = wave_sum(sq[1]) * (1.f / D);
#pragma unroll
        for (int bb = 0; bb < 2; ++bb) {
          const float mb = rbf(sm[bb]), rden = __builtin_amdgcn_rcpf(rbf(__builtin_amdgcn_sqrtf(rbf(rbf(sq[bb]) + 1e-5f))));
#pragma unroll
          for (int i = 0; i < 5; ++i) S.xnp[bb][lane * 5 + i] = pk2(rbf(xv[bb][2 * i] - mb) * rden, rbf(xv[bb][2 * i + 1] - mb) * rden);
        }
      }
      asm volatile("s_waitcnt lgkmcnt(0)" ::: "memory");
      float a[2][8];
#pragma unroll
      for (int k = 0; k < 8; ++k) { a[0][k] = 0.f; a[1][k] = 0.f; }
#pragma unroll
      for (int i4 = 0; i4 < 2; ++i4) {
        const u32x4 x0 = *(const u32x4*)&S.xnp[0][jr * 64 + slot * 8 + i4 * 4];
        const u32x4 x1 = *(const u32x4*)&S.xnp[1][jr * 64 + slot * 8 + i4 * 4];
#pragma unroll
        for (int i = 0; i < 4; ++i) {
          const u32x4 w0 = wqr[(i4 * 4 + i) * 2], w1 = wqr[(i4 * 4 + i) * 2 + 1];
#pragma unroll
          for (int k = 0; k < 4; ++k) {
            const u32 wl = plo(w0[k], w1[k]), wh = phi(w0[k], w1[k]);
            a[0][2 * k] = dot2(wl, x0[i], a[0][2 * k]); a[0][2 * k + 1] = dot2(wh, x0[i], a[0][2 * k + 1]);
            a[1][2 * k] = dot2(wl, x1[i], a[1][2 * k]); a[1][2 * k + 1] = dot2(wh, x1[i], a[1][2 * k + 1]);
          }
        }
      }
      if (st < 192) {
#pragma unroll
        for (int bb = 0; bb < 2; ++bb) {
          *(f32x4*)&S.qpart[slot][bb][cc * 8] = f32x4{a[bb][0], a[bb][1], a[bb][2], a[bb][3]};
          *(f32x4*)&S.qpart[slot][bb][cc * 8 + 4] = f32x4{a[bb][4], a[bb][5], a[bb][6], a[bb][7]};
        }
      }
      asm volatile("s_waitcnt lgkmcnt(0)" ::: "memory");
      if (lane == 0) __hip_atomic_fetch_add(&S.qcnt, 1u, __ATOMIC_RELAXED, __HIP_MEMORY_SCOPE_WORKGROUP);
      while (LDS_LD(&S.qcnt) != 4u) __builtin_amdgcn_s_sleep(1);
      asm volatile("" ::: "memory");
      GET_TAGS();
      if (st < 192) {
        const int bb = st / 96, k = st % 96;
        float s0 = 0.f, s1 = 0.f;
#pragma unroll
        for (int w = 0; w < 8; ++w) { const f32x2 t = *(const f32x2*)&S.qpart[w][bb][2 * k]; s0 += t[0]; s1 += t[1]; }
        st_gran(P.ws + WS_Q + ((size_t)((h * 2 + bb) * 5 + jr) * 96 + k) * 16, s0, s1, ta, tb);
      }
    }
    if (wave == 8) {
      const unsigned char* pq = P.ws + WS_Q + (size_t)(h * 2 + b) * 480 * 16;
      const unsigned char* p0 = pq + lane * 16;
      const unsigned char* p1 = pq + (64 + c) * 16;
      u32x4 g[10];
      GET_TAGS();
      for (int it = 0; it < SPIN_LIMIT; ++it) {
        ld_gran10(p0, p0 + 4608, p1, p1 + 4608, g);
        bool ok = true;
#pragma unroll
        for (int i = 0; i < 10; ++i) ok = ok && TAGOK(g[i]);
        if (__all(ok)) break;
        __builtin_amdgcn_s_sleep(2);
      }
      float s0 = 0.f, s1 = 0.f, t0 = 0.f, t1 = 0.f;
#pragma unroll
      for (int i = 0; i < 5; ++i) {
        s0 += __uint_as_float(g[i][0]); s1 += __uint_as_float(g[i][2]);
        t0 += __uint_as_float(g[5 + i][0]); t1 += __uint_as_float(g[5 + i][2]);
      }
      S.qpk[lane] = pk2(s0, s1);
      if (lane < 32) S.qpk[64 + lane] = pk2(t0, t1);
      asm volatile("s_waitcnt lgkmcnt(0)" ::: "memory");
      if (lane == 0) __hip_atomic_store(&S.qflag, 1u, __ATOMIC_RELAXED, __HIP_MEMORY_SCOPE_WORKGROUP);
    } else {
      __builtin_amdgcn_s_setprio(3);
      const int tw = st - 64;
      if (isO) {
        const __amdgpu_buffer_rsrc_t ro = mkrsrc(P.wo + (size_t)(h * DH + jr * 32) * D, 32 * D * 2);
        u32x4 w0[7], w1[7];
#pragma unroll
        for (int i = 0; i < 7; ++i) {
          const int task = tw + 192 * i, k = task / 80, ch = task % 80;
          w0[i] = __builtin_amdgcn_raw_buffer_load_b128(ro, (u32)(2 * k) * (D * 2) + ch * 16, 0, 2);
          w1[i] = __builtin_amdgcn_raw_buffer_load_b128(ro, (u32)(2 * k) * (D * 2) + ch * 16, D * 2, 2);
        }
#pragma unroll
        for (int i = 0; i < 7; ++i) {
          const int task = tw + 192 * i, k = task / 80, ch = task % 80;
          if (task < 1280) {
            u32* d = &S.wst[k * 640 + ch * 8];
            *(u32x4*)d = u32x4{plo(w0[i][0], w1[i][0]), phi(w0[i][0], w1[i][0]), plo(w0[i][1], w1[i][1]), phi(w0[i][1], w1[i][1])};
            *(u32x4*)(d + 4) = u32x4{plo(w0[i][2], w1[i][2]), phi(w0[i][2], w1[i][2]), plo(w0[i][3], w1[i][3]), phi(w0[i][3], w1[i][3])};
          }
        }
      } else {
        const __amdgpu_buffer_rsrc_t rd = mkrsrc(P.wd + (size_t)(fidx * 24) * D, 24 * D * 2);
        u32x4 w0[5], w1[5];
#pragma unroll
        for (int i = 0; i < 5; ++i) {
          const int task = tw + 192 * i, k = task / 80, ch = task % 80;
          w0[i] = __builtin_amdgcn_raw_buffer_load_b128(rd, (u32)(2 * k) * (D * 2) + ch * 16, 0, 2);
          w1[i] = __builtin_amdgcn_raw_buffer_load_b128(rd, (u32)(2 * k) * (D * 2) + ch * 16, D * 2, 2);
        }
#pragma unroll
        for (int i = 0; i < 5; ++i) {
          const int task = tw + 192 * i, k = task / 80, ch = task % 80;
          u32* d = &S.wst[48 * GUS + k * 640 + ch * 8];
          *(u32x4*)d = u32x4{plo(w0[i][0], w1[i][0]), phi(w0[i][0], w1[i][0]), plo(w0[i][1], w1[i][1]), phi(w0[i][1], w1[i][1])};
          *(u32x4*)(d + 4) = u32x4{plo(w0[i][2], w1[i][2]), phi(w0[i][2], w1[i][2]), plo(w0[i][3], w1[i][3]), phi(w0[i][3], w1[i][3])};
        }
      }
    }
  } else {
    stream_body(wave, 16, wave * 64, true);
  }
  __syncthreads();
  GET_TAGS();
  float hres0 = 0.f, hres1 = 0.f;
  const int t3 = tid < 320 ? tid : 319;
  const int gslot = (tid >> 2) * 8 + (tid & 3);
  u32x2 wpre[16];
  if (wave < 2) {
    float mm[8];
    float Mwg = -INFINITY;
#pragma unroll
    for (int w = 0; w < 8; ++w) { mm[w] = S.ml[w][0]; Mwg = fmaxf(Mwg, mm[w]); }
    float Lwg = 0.f;
#pragma unroll
    for (int w = 0; w < 8; ++w) { mm[w] = __builtin_amdgcn_exp2f(mm[w] - Mwg); Lwg = fmaf(mm[w], S.ml[w][1], Lwg); }
    const int t = tid < 97 ? (tid > 0 ? tid - 1 : 0) : 95;
    float o0 = 0.f, o1 = 0.f;
#pragma unroll
    for (int w = 0; w < 8; ++w) {
      const f32x2 a = *(const f32x2*)&S.red[2 * w][2 * t], bb = *(const f32x2*)&S.red[2 * w + 1][2 * t];
      o0 = fmaf(mm[w], a[0] + bb[0], o0); o1 = fmaf(mm[w], a[1] + bb[1], o1);
    }
    if (tid == 0) { o0 = Mwg; o1 = Lwg; }
    if (tid < 97) {
      st_gran(P.ws + WS_O + ((size_t)prod * 128 + tid) * 16, o0, o1, ta, tb);
      *(f32x2*)&S.oown[tid * 2] = f32x2{o0, o1};
    }
    asm volatile("s_waitcnt lgkmcnt(0)" ::: "memory");
    if (lane == 0) __hip_atomic_fetch_add(&S.ocnt, 1u, __ATOMIC_RELAXED, __HIP_MEMORY_SCOPE_WORKGROUP);
  } else if (isO && wave < 4) {
    const int bb = wave - 2, pj = bb * 4 + r4;
    const bool own = pj == j8;
    const unsigned char* pb = P.ws + WS_O + (size_t)(h * 8 + pj) * 128 * 16;
    u32x4 go, gm;
    for (int it = 0; it < SPIN_LIMIT; ++it) {
      ld_gran2(pb + (1 + jr * 16 + c16) * 16, pb, go, gm);
      if (__all(own || (TAGOK(go) && TAGOK(gm)))) break;
    }
    while (LDS_LD(&S.ocnt) != 2u) __builtin_amdgcn_s_sleep(1);
    asm volatile("" ::: "memory");
    const f32x2 om = *(const f32x2*)&S.oown[0], ov = *(const f32x2*)&S.oown[(1 + jr * 16 + c16) * 2];
    const float m = own ? om[0] : __uint_as_float(gm[0]), l = own ? om[1] : __uint_as_float(gm[2]);
    const float v0 = own ? ov[0] : __uint_as_float(go[0]), v1 = own ? ov[1] : __uint_as_float(go[2]);
    const float M = x1632max(m);
    const float e = __builtin_amdgcn_exp2f(m - M);
    const float L = x1632sum(e * l);
    const float a0 = x1632sum(e * v0), a1 = x1632sum(e * v1);
    const float rl = __builtin_amdgcn_rcpf(L);
    if (lane < 16) S.opk[bb][lane] = pk2(a0 * rl, a1 * rl);
    asm volatile("s_waitcnt lgkmcnt(0)" ::: "memory");
    if (lane == 0) __hip_atomic_fetch_add(&S.oflag, 1u, __ATOMIC_RELAXED, __HIP_MEMORY_SCOPE_WORKGROUP);
  } else if (isO && wave >= 7) {
    const int yt = tid - 448;
#pragma unroll
    for (int k = 0; k < 16; ++k) wpre[k] = *(const u32x2*)&S.wst[k * 640 + 2 * yt];
    while (LDS_LD(&S.oflag) != 2u) __builtin_amdgcn_s_sleep(1);
    asm volatile("" ::: "memory");
    float y00 = 0.f, y01 = 0.f, y10 = 0.f, y11 = 0.f;
#pragma unroll
    for (int k4 = 0; k4 < 4; ++k4) {
      const u32x4 oa = *(const u32x4*)&S.opk[0][k4 * 4], ob = *(const u32x4*)&S.opk[1][k4 * 4];
#pragma unroll
      for (int k = 0; k < 4; ++k) {
        const u32x2 w = wpre[k4 * 4 + k];
        y00 = dot2(w[0], oa[k], y00); y01 = dot2(w[1], oa[k], y01);
        y10 = dot2(w[0], ob[k], y10); y11 = dot2(w[1], ob[k], y11);
      }
    }
    unsigned char* py = P.ws + WS_Y + ((size_t)(h * 6 + jr) * 640 + (yt >> 2) * 8 + (yt & 3)) * 16;
    st_gran(py, y00, y01, ta, tb);
    st_gran(py + 64, y10, y11, ta, tb);
    if (lane == 0) __hip_atomic_fetch_add(&S.ycnt, 1u, __ATOMIC_RELAXED, __HIP_MEMORY_SCOPE_WORKGROUP);
  }
  if (isR && wave < 6) {
    const unsigned char* p0 = P.ws + WS_Y + ((size_t)(tid >> 3) * 640 + rid * 8 + (tid & 7)) * 16;
    constexpr size_t ST = (size_t)48 * 640 * 16;
    u32x4 ga, gb, gc, gd;
    while (LDS_LD(&S.ycnt) != 5u) __builtin_amdgcn_s_sleep(2);
    for (int it = 0; it < SPIN_LIMIT; ++it) {
      ld_gran4(p0, p0 + ST, p0 + 2 * ST, p0 + 3 * ST, ga, gb, gc, gd);
      if (__all(TAGOK(ga) && TAGOK(gb) && TAGOK(gc) && TAGOK(gd))) break;
      __builtin_amdgcn_s_sleep(1);
    }
    float y0 = (__uint_as_float(ga[0]) + __uint_as_float(gb[0])) + (__uint_as_float(gc[0]) + __uint_as_float(gd[0]));
    float y1 = (__uint_as_float(ga[2]) + __uint_as_float(gb[2])) + (__uint_as_float(gc[2]) + __uint_as_float(gd[2]));
    y0 += dpp<XOR8>(y0); y1 += dpp<XOR8>(y1);
    y0 = x1632sum(y0); y1 = x1632sum(y1);
    if (lane < 8) *(f32x2*)&S.wsum[wave][lane * 2] = f32x2{y0, y1};
    asm volatile("s_waitcnt lgkmcnt(0)" ::: "memory");
    if (lane == 0) __hip_atomic_fetch_add(&S.hcnt, 1u, __ATOMIC_RELAXED, __HIP_MEMORY_SCOPE_WORKGROUP);
    if (wave == 0) {
      while (LDS_LD(&S.hcnt) != 6u) __builtin_amdgcn_s_sleep(1);
      asm volatile("" ::: "memory");
      if (tid < 8) {
        float a0 = 0.f, a1 = 0.f;
#pragma unroll
        for (int w = 0; w < 6; ++w) { a0 += S.wsum[w][tid * 2]; a1 += S.wsum[w][tid * 2 + 1]; }
        hres0 = rbf(lo2f(xres) + rbf(a0)); hres1 = rbf(hi2f(xres) + rbf(a1));
        st_gran(P.ws + WS_H + (size_t)(rid * 8 + tid) * 16, hres0, hres1, ta, tb);
      }
      if (lane == 0) __hip_atomic_store(&S.hflag, 1u, __ATOMIC_RELAXED, __HIP_MEMORY_SCOPE_WORKGROUP);
    }
  }
  if (isF) {
    if (wave >= 8) {
      while (LDS_LD(&S.ocnt) != 2u) __builtin_amdgcn_s_sleep(1);
      asm volatile("" ::: "memory");
      const int st = tid - 512;
#pragma unroll
      for (int m = 0; m < 2; ++m) {
        {
          const __amdgpu_buffer_rsrc_t ru = mkrsrc((m ? P.wu : P.wg) + fidx * 24, (D * F - fidx * 24) * 2);
          u32x4 a0[4], a1[4];
#pragma unroll
          for (int i = 0; i < 4; ++i) {
            const int task = st + 256 * i, k = task / 3, q = task % 3;
            const u32 vo = task < 960 ? (u32)(2 * k) * (F * 2) + q * 16 : 0x7fffff00u;
            a0[i] = __builtin_amdgcn_raw_buffer_load_b128(ru, vo, 0, 0);
            a1[i] = __builtin_amdgcn_raw_buffer_load_b128(ru, vo, F * 2, 0);
          }
#pragma unroll
          for (int i = 0; i < 4; ++i) {
            const int task = st + 256 * i, k = task / 3, q = task % 3;
            if (task < 960) {
#pragma unroll
              for (int e = 0; e < 4; ++e) {
                S.wst[(m * 24 + q * 8 + 2 * e) * GUS + k] = plo(a0[i][e], a1[i][e]);
                S.wst[(m * 24 + q * 8 + 2 * e + 1) * GUS + k] = phi(a0[i][e], a1[i][e]);
              }
            }
          }
        }
      }
    }
    __syncthreads();
    u32x4 wreg[5];
    {
      const u32* wrow = &S.wst[((r4 >> 1) * 24 + 2 * wave + (r4 & 1)) * GUS + 20 * c16];
#pragma unroll
      for (int i = 0; i < 5; ++i) wreg[i] = *(const u32x4*)&wrow[4 * i];
#pragma unroll
      for (int k = 0; k < 12; ++k) wpre[k] = *(const u32x2*)&S.wst[48 * GUS + k * 640 + 2 * t3];
    }
    float cs = 0.f;
#pragma unroll
    for (int i = 0; i < 5; ++i)
#pragma unroll
      for (int k = 0; k < 4; ++k) cs = dot2(wreg[i][k], 0x3f803f80u, cs);
    cs = row16sum(cs);
    if (tid < 320) {
      const unsigned char* p0 = P.ws + WS_H + (size_t)gslot * 16;
      u32x4 ga, gb;
      for (int it = 0; it < SPIN_LIMIT; ++it) {
        ld_gran2(p0, p0 + 64, ga, gb);
        if (__all(TAGOK(ga) && TAGOK(gb))) break;
      }
      const float h00 = __uint_as_float(ga[0]), h01 = __uint_as_float(ga[2]), h10 = __uint_as_float(gb[0]), h11 = __uint_as_float(gb[2]);
      S.hp[0][tid] = (ga[0] >> 16) | (ga[2] & 0xffff0000u);
      S.hp[1][tid] = (gb[0] >> 16) | (gb[2] & 0xffff0000u);
      float s0 = h00 + h01, q0 = h00 * h00 + h01 * h01, s1 = h10 + h11, q1 = h10 * h10 + h11 * h11;
      s0 = sw32sum(s0, s1); q0 = sw32sum(q0, q1);
      s0 = sw16sum(s0, q0);
      s0 = row16sum(s0);
      if (c16 == 0) S.st[wave][r4] = s0;
    }
    __syncthreads();
    {
      const int bsel = c16 & 1;
      float d0 = 0.f, d1 = 0.f;
#pragma unroll
      for (int i = 0; i < 5; ++i) {
        const u32x4 ha = *(const u32x4*)&S.hp[0][20 * c16 + 4 * i], hb = *(const u32x4*)&S.hp[1][20 * c16 + 4 * i];
#pragma unroll
        for (int k = 0; k < 4; ++k) { d0 = dot2(wreg[i][k], ha[k], d0); d1 = dot2(wreg[i][k], hb[k], d1); }
      }
      float sm = 0.f, sq = 0.f;
#pragma unroll
      for (int w = 0; w < 5; ++w) { const f32x2 t = *(const f32x2*)&S.st[w][bsel * 2]; sm += t[0]; sq += t[1]; }
      const float mean = sm * (1.f / D);
      const float var = fmaxf(sq * (1.f / D) - mean * mean, 0.f);
      const float mb = rbf(mean), den = rbf(__builtin_amdgcn_sqrtf(rbf(rbf(var) + 1e-5f)));
      const float rden = __builtin_amdgcn_rcpf(den);
      float dd = (bsel ? d1 : d0) + dpp<XOR1>(bsel ? d0 : d1);
      dd += dpp<XOR2>(dd); dd += dpp<XOR1>(dpp<XOR7>(dd)); dd += dpp<XOR8>(dd);
      const float mine = rbf((dd - mb * cs) * rden);
      const float up = upper32(mine);
      const float t = 0.7978845608028654f * (mine + 0.044715f * mine * mine * mine);
      const float th = 1.f - 2.f * __builtin_amdgcn_rcpf(__builtin_amdgcn_exp2f(t * 2.8853900817779268f) + 1.f);
      const float gval = rbf(rbf(0.5f * mine * (1.f + th)) * up);
      if (half == 0 && c16 < 2) ((uint16_t*)&S.gp[c16][0])[2 * wave + r4] = (uint16_t)(__float_as_uint(gval) >> 16);
    }
    __syncthreads();
    if (tid < 320) {
      float z00 = 0.f, z01 = 0.f, z10 = 0.f, z11 = 0.f;
#pragma unroll
      for (int k4 = 0; k4 < 3; ++k4) {
        const u32x4 oa = *(const u32x4*)&S.gp[0][k4 * 4], ob = *(const u32x4*)&S.gp[1][k4 * 4];
#pragma unroll
        for (int k = 0; k < 4; ++k) {
          const u32x2 w = wpre[k4 * 4 + k];
          z00 = dot2(w[0], oa[k], z00); z01 = dot2(w[1], oa[k], z01);
          z10 = dot2(w[0], ob[k], z10); z11 = dot2(w[1], ob[k], z11);
        }
      }
      unsigned char* pz = P.ws + WS_Z + ((size_t)fidx * 640 + gslot) * 16;
      st_gran(pz, z00, z01, ta, tb);
      st_gran(pz + 64, z10, z11, ta, tb);
    }
  }
  if (isR && wave < 9) {
    if (wave > 0) {
      const int zt = tid - 64;
      const unsigned char* p0 = P.ws + WS_Z + ((size_t)(zt >> 3) * 640 + rid * 8 + (zt & 7)) * 16;
      u32x4 ga;
      while (LDS_LD(&S.hflag) == 0u) __builtin_amdgcn_s_sleep(2);
      for (int it = 0; it < SPIN_LIMIT; ++it) {
        ld_gran1(p0, ga);
        if (__all(TAGOK(ga))) break;
        __builtin_amdgcn_s_sleep(1);
      }
      float y0 = __uint_as_float(ga[0]), y1 = __uint_as_float(ga[2]);
      y0 += dpp<XOR8>(y0); y1 += dpp<XOR8>(y1);
      y0 = x1632sum(y0); y1 = x1632sum(y1);
      if (lane < 8) *(f32x2*)&S.zsum[wave - 1][lane * 2] = f32x2{y0, y1};
      asm volatile("s_waitcnt lgkmcnt(0)" ::: "memory");
      if (lane == 0) __hip_atomic_fetch_add(&S.zcnt, 1u, __ATOMIC_RELAXED, __HIP_MEMORY_SCOPE_WORKGROUP);
    } else {
      while (LDS_LD(&S.zcnt) != 8u) __builtin_amdgcn_s_sleep(1);
      asm volatile("" ::: "memory");
      if (tid < 8) {
        float a0 = 0.f, a1 = 0.f;
#pragma unroll
        for (int w = 0; w < 8; ++w) { a0 += S.zsum[w][tid * 2]; a1 += S.zsum[w][tid * 2 + 1]; }
        const u32 o = pk2(hres0 + rbf(a0), hres1 + rbf(a1));
        ((u32*)(P.out + (tid >> 2) * D + rid * 8))[tid & 3] = o;
      }
      if (wg == 0 && tid == 0) {
        u32x2 ne = {ta, tb};
        asm volatile("global_store_dwordx2 %0, %1, off sc1" :: "v"(P.ws + WS_EPOCH), "v"(ne) : "memory");
      }
    }
  }
}
}

extern "C" void kernel_launch(void* const* d_in, const int* in_sizes, int n_in, void* d_out, int out_size,
                              void* d_ws, size_t ws_size, hipStream_t stream) {
  Params P;
  P.x = (const uint16_t*)d_in[0]; P.kc = (const uint16_t*)d_in[1]; P.vc = (const uint16_t*)d_in[2];
  P.wq = (const uint16_t*)d_in[3]; P.wo = (const uint16_t*)d_in[4];
  P.wg = (const uint16_t*)d_in[6]; P.wu = (const uint16_t*)d_in[7]; P.wd = (const uint16_t*)d_in[8];
  P.out = (uint16_t*)d_out; P.ws = (unsigned char*)d_ws;
  fused_kernel<<<NWG, NT, 0, stream>>>(P);
}
```

```cpp
#include <hip/hip_runtime.h>
#include <hip/hip_bf16.h>
#include <stdint.h>

namespace {
typedef unsigned u32;
typedef u32 u32x2 __attribute__((ext_vector_type(2)));
typedef u32 u32x4 __attribute__((ext_vector_type(4)));
typedef float f32x2 __attribute__((ext_vector_type(2)));
typedef float f32x4 __attribute__((ext_vector_type(4)));
typedef __bf16 bf16x2 __attribute__((ext_vector_type(2)));

constexpr int D = 640, H = 32, SK = 8192, DH = 192, W = 2048, F = 1536, QN = H * DH;
constexpr int NWG = 256, NT = 768, NRED = 80;
constexpr int SPIN_LIMIT = 1 << 20;

constexpr size_t WS_EPOCH = 0;
constexpr size_t WS_Q = 4096;
constexpr size_t WS_O = WS_Q + (size_t)32 * 2 * 480 * 16;
constexpr size_t WS_H = WS_O + (size_t)256 * 128 * 16;
constexpr size_t WS_Y = WS_H + 640 * 16;
constexpr size_t WS_Z = WS_Y + (size_t)192 * 640 * 16;
constexpr size_t WS_END = WS_Z + (size_t)64 * 640 * 16;

__device__ __forceinline__ float rbf(float f) { return __bfloat162float(__float2bfloat16(f)); }
__device__ __forceinline__ float lo2f(u32 v) { return __uint_as_float(v << 16); }
__device__ __forceinline__ float hi2f(u32 v) { return __uint_as_float(v & 0xffff0000u); }
__device__ __forceinline__ u32 pk2(float lo, float hi) {
  u32 r; asm("v_cvt_pk_bf16_f32 %0, %1, %2" : "=v"(r) : "v"(lo), "v"(hi)); return r;
}
__device__ __forceinline__ float dot2(u32 a, u32 b, float c) {
  return __builtin_amdgcn_fdot2_f32_bf16(__builtin_bit_cast(bf16x2, a), __builtin_bit_cast(bf16x2, b), c, false);
}
__device__ __forceinline__ u32 plo(u32 w0, u32 w1) { return __builtin_amdgcn_perm(w1, w0, 0x05040100); }
__device__ __forceinline__ u32 phi(u32 w0, u32 w1) { return __builtin_amdgcn_perm(w1, w0, 0x07060302); }
template <int CTRL> __device__ __forceinline__ float dpp(float x) {
  return __builtin_bit_cast(float, __builtin_amdgcn_mov_dpp(__builtin_bit_cast(int, x), CTRL, 0xf, 0xf, true));
}
constexpr int XOR1 = 0xB1, XOR2 = 0x4E, XOR7 = 0x141, XOR8 = 0x128;
__device__ __forceinline__ float sw16sum(float a, float b) {
  auto s = __builtin_amdgcn_permlane16_swap(__float_as_uint(a), __float_as_uint(b), false, false);
  return __uint_as_float(s[0]) + __uint_as_float(s[1]);
}
__device__ __forceinline__ float sw32sum(float a, float b) {
  auto s = __builtin_amdgcn_permlane32_swap(__float_as_uint(a), __float_as_uint(b), false, false);
  return __uint_as_float(s[0]) + __uint_as_float(s[1]);
}
__device__ __forceinline__ float upper32(float a) {
  auto s = __builtin_amdgcn_permlane32_swap(__float_as_uint(a), __float_as_uint(a), false, false);
  return __uint_as_float(s[1]);
}
__device__ __forceinline__ float x1632max(float x) {
  auto s = __builtin_amdgcn_permlane16_swap(__float_as_uint(x), __float_as_uint(x), false, false);
  x = fmaxf(__uint_as_float(s[0]), __uint_as_float(s[1]));
  auto t = __builtin_amdgcn_permlane32_swap(__float_as_uint(x), __float_as_uint(x), false, false);
  return fmaxf(__uint_as_float(t[0]), __uint_as_float(t[1]));
}
__device__ __forceinline__ float x1632sum(float x) { x = sw16sum(x, x); return sw32sum(x, x); }
__device__ __forceinline__ float row16sum(float x) {
  x += dpp<XOR1>(x); x += dpp<XOR2>(x); x += dpp<XOR7>(x); x += dpp<XOR8>(x); return x;
}
__device__ __forceinline__ float wave_sum(float x) { return x1632sum(row16sum(x)); }
__device__ __forceinline__ float wave_max(float x) {
  x = fmaxf(x, dpp<XOR1>(x)); x = fmaxf(x, dpp<XOR2>(x)); x = fmaxf(x, dpp<XOR7>(x)); x = fmaxf(x, dpp<XOR8>(x));
  return x1632max(x);
}
__device__ __forceinline__ void st_gran(unsigned char* p, float v0, float v1, u32 ta, u32 tb) {
  u32x4 g = {__float_as_uint(v0), ta, __float_as_uint(v1), tb};
  asm volatile("global_store_dwordx4 %0, %1, off sc1\n\ts_nop 1" :: "v"(p), "v"(g) : "memory");
}
__device__ __forceinline__ void ld_gran1(const unsigned char* p, u32x4& a) {
  asm volatile("global_load_dwordx4 %0, %1, off sc1\n\ts_waitcnt vmcnt(0)" : "=&v"(a) : "v"(p) : "memory");
}
__device__ __forceinline__ void ld_gran2(const unsigned char* p, const unsigned char* q, u32x4& a, u32x4& b) {
  asm volatile("global_load_dwordx4 %0, %2, off sc1\n\tglobal_load_dwordx4 %1, %3, off sc1\n\ts_waitcnt vmcnt(0)"
               : "=&v"(a), "=&v"(b) : "v"(p), "v"(q) : "memory");
}
__device__ __forceinline__ void ld_gran4(const unsigned char* p0, const unsigned char* p1, const unsigned char* p2,
                                         const unsigned char* p3, u32x4& a, u32x4& b, u32x4& c, u32x4& d) {
  asm volatile("global_load_dwordx4 %0, %4, off sc1\n\tglobal_load_dwordx4 %1, %5, off sc1\n\t"
               "global_load_dwordx4 %2, %6, off sc1\n\tglobal_load_dwordx4 %3, %7, off sc1\n\ts_waitcnt vmcnt(0)"
               : "=&v"(a), "=&v"(b), "=&v"(c), "=&v"(d) : "v"(p0), "v"(p1), "v"(p2), "v"(p3) : "memory");
}
__device__ __forceinline__ void ld_gran10(const unsigned char* p0, const unsigned char* p0b, const unsigned char* p1,
                                          const unsigned char* p1b, u32x4 (&g)[10]) {
  asm volatile(
      "global_load_dwordx4 %0, %10, off sc1\n\tglobal_load_dwordx4 %1, %10, off offset:1536 sc1\n\t"
      "global_load_dwordx4 %2, %10, off offset:3072 sc1\n\tglobal_load_dwordx4 %3, %11, off sc1\n\t"
      "global_load_dwordx4 %4, %11, off offset:1536 sc1\n\t"
      "global_load_dwordx4 %5, %12, off sc1\n\tglobal_load_dwordx4 %6, %12, off offset:1536 sc1\n\t"
      "global_load_dwordx4 %7, %12, off offset:3072 sc1\n\tglobal_load_dwordx4 %8, %13, off sc1\n\t"
      "global_load_dwordx4 %9, %13, off offset:1536 sc1\n\ts_waitcnt vmcnt(0)"
      : "=&v"(g[0]), "=&v"(g[1]), "=&v"(g[2]), "=&v"(g[3]), "=&v"(g[4]), "=&v"(g[5]), "=&v"(g[6]), "=&v"(g[7]),
        "=&v"(g[8]), "=&v"(g[9])
      : "v"(p0), "v"(p0b), "v"(p1), "v"(p1b) : "memory");
}
__device__ __forceinline__ __amdgpu_buffer_rsrc_t mkrsrc(const void* p, u32 bytes) {
  return __builtin_amdgcn_make_buffer_rsrc(const_cast<void*>(p), 0, bytes, 0x00020000);
}
#define TAGOK(g) ((g)[1] == ta && (g)[3] == tb)
#define LDS_LD(p) __hip_atomic_load((p), __ATOMIC_RELAXED, __HIP_MEMORY_SCOPE_WORKGROUP)

struct Params {
  const uint16_t* x; const uint16_t* kc; const uint16_t* vc; const uint16_t* wq; const uint16_t* wo;
  const uint16_t* wg; const uint16_t* wu; const uint16_t* wd; uint16_t* out; unsigned char* ws;
};

constexpr int GUS = 324;
struct Lds {
  u32 wst[48 * GUS + 12 * 640];
  u32 xnp[2][320];
  float qpart[8][2][192];
  u32 qpk[96];
  float pl[8][64];
  float ml[8][2];
  float red[16][192];
  u32 opk[2][16];
  float wsum[8][16];
  float st[5][4];
  u32 hp[2][320];
  u32 gp[2][12];
  float oown[200];
  float zsum[8][16];
  u32 qcnt, qflag, ocnt, oflag, hcnt, zcnt, ycnt, hflag;
};

__global__ void __launch_bounds__(NT, 3) fused_kernel(Params P) {
  __shared__ __attribute__((aligned(16))) Lds S;
  const int tid = threadIdx.x, lane = tid & 63, wave = __builtin_amdgcn_readfirstlane(tid >> 6);
  const int half = lane >> 5, c = lane & 31;
  const int r4 = lane >> 4, c16 = lane & 15;
  const int wg = blockIdx.x, xcd = wg & 7, idx = wg >> 3;
  const int h = (idx >> 3) * 8 + xcd, j8 = idx & 7, b = j8 >> 2, sp = j8 & 3;
  const int prod = h * 8 + j8;
  const bool isF = sp == 3, isO = !isF;
  const int jr = j8 - b;
  const bool isQ = isO && jr < 5;
  const int fidx = xcd * 8 + (idx >> 3) * 2 + b;
  const int rid = h * 5 + jr;
  const bool isR = isQ && rid < NRED;

  if (tid == 0) { S.qcnt = 0u; S.qflag = 0u; S.ocnt = 0u; S.oflag = 0u; S.hcnt = 0u; S.zcnt = 0u; S.ycnt = 0u; S.hflag = 0u; }
  __syncthreads();
  if (wave >= 8) __builtin_amdgcn_s_setprio(3);
  unsigned long long ep = __hip_atomic_load((const unsigned long long*)(P.ws + WS_EPOCH), __ATOMIC_RELAXED, __HIP_MEMORY_SCOPE_AGENT);
  u32 ta, tb;
#define GET_TAGS() do { asm volatile("" : "+v"(ep)); ta = (u32)ep + 1u; tb = (u32)(ep >> 32) + 1u; } while (0)
  const u32 xres = ((const u32*)(P.x + ((tid >> 2) & 1) * D + (rid % NRED) * 8))[tid & 3];
  auto stream_body = [&](const int slotw, const int ng, const int row0, const bool waitq) {
    const size_t kvoff = ((size_t)(b * H + h) * SK + (SK - W) + sp * 512 + row0) * DH;
    const __amdgpu_buffer_rsrc_t krs = mkrsrc(P.kc + kvoff, ng * 1536);
    const __amdgpu_buffer_rsrc_t vrs = mkrsrc(P.vc + kvoff, ng * 1536);
    const u32 loA = r4 * 384 + c16 * 16, loB = r4 * 384 + 256 + c16 * 8;
    u32x4 ka[16]; u32x2 kb[16];
#pragma unroll
    for (int i = 0; i < 16; ++i) {
      ka[i] = __builtin_amdgcn_raw_buffer_load_b128(krs, loA, i * 1536, 2);
      kb[i] = __builtin_amdgcn_raw_buffer_load_b64(krs, loB, i * 1536, 2);
    }
    u32x4 va[16]; u32x2 vb[16];
#pragma unroll
    for (int i = 0; i < 8; ++i) {
      va[i] = __builtin_amdgcn_raw_buffer_load_b128(vrs, loA, i * 1536, 2);
      vb[i] = __builtin_amdgcn_raw_buffer_load_b64(vrs, loB, i * 1536, 2);
    }
    __builtin_amdgcn_sched_barrier(0);
    if (waitq) { while (LDS_LD(&S.qflag) == 0u) __builtin_amdgcn_s_sleep(1); }
    asm volatile("" ::: "memory");
    const u32x4 qa = *(const u32x4*)&S.qpk[4 * c16];
    const u32x2 qb = *(const u32x2*)&S.qpk[64 + 2 * c16];
    float d[16];
#pragma unroll
    for (int hh = 0; hh < 2; ++hh) {
#pragma unroll
      for (int i = hh * 8; i < hh * 8 + 8; ++i) {
        float t = dot2(ka[i][0], qa[0], 0.f);
        t = dot2(ka[i][1], qa[1], t); t = dot2(ka[i][2], qa[2], t); t = dot2(ka[i][3], qa[3], t);
        t = dot2(kb[i][0], qb[0], t); d[i] = dot2(kb[i][1], qb[1], t);
      }
      __builtin_amdgcn_sched_barrier(0);
#pragma unroll
      for (int i = 8 + hh * 4; i < 12 + hh * 4; ++i) {
        va[i] = __builtin_amdgcn_raw_buffer_load_b128(vrs, loA, i * 1536, 2);
        vb[i] = __builtin_amdgcn_raw_buffer_load_b64(vrs, loB, i * 1536, 2);
      }
      __builtin_amdgcn_sched_barrier(0);
    }
#define TSTEP(A, N, CTRL, BIT)                                                                 \
  _Pragma("unroll") for (int i = 0; i < N; ++i) {                                              \
    const bool hb = (c & BIT) != 0;                                                            \
    const float keep = hb ? A[i + N] : A[i], send = hb ? A[i] : A[i + N];                      \
    A[i] = keep + dpp<CTRL>(send);                                                             \
  }
    TSTEP(d, 8, XOR8, 8) TSTEP(d, 4, XOR7, 4) TSTEP(d, 2, XOR2, 2) TSTEP(d, 1, XOR1, 1)
    const float sc = c16 < ng ? rbf(rbf(d[0]) * 0.07216878364870322f) * 1.4426950408889634f : -INFINITY;
    const float mw = wave_max(sc);
    const float p = __builtin_amdgcn_exp2f(sc - mw);
    const float lw = wave_sum(p);
    S.pl[slotw][lane] = p;
    if (lane == 0) { S.ml[slotw][0] = mw; S.ml[slotw][1] = lw; }
    asm volatile("s_waitcnt lgkmcnt(0)" ::: "memory");
    float acc[12];
#pragma unroll
    for (int k = 0; k < 12; ++k) acc[k] = 0.f;
#pragma unroll
    for (int i4 = 0; i4 < 4; ++i4) {
      const f32x4 pp = *(const f32x4*)&S.pl[slotw][r4 * 16 + i4 * 4];
#pragma unroll
      for (int j = 0; j < 4; ++j) {
        const u32x4 v = va[i4 * 4 + j]; const u32x2 w = vb[i4 * 4 + j];
#pragma unroll
        for (int k = 0; k < 4; ++k) {
          acc[2 * k] = fmaf(pp[j], lo2f(v[k]), acc[2 * k]);
          acc[2 * k + 1] = fmaf(pp[j], hi2f(v[k]), acc[2 * k + 1]);
        }
#pragma unroll
        for (int k = 0; k < 2; ++k) {
          acc[8 + 2 * k] = fmaf(pp[j], lo2f(w[k]), acc[8 + 2 * k]);
          acc[8 + 2 * k + 1] = fmaf(pp[j], hi2f(w[k]), acc[8 + 2 * k + 1]);
        }
      }
    }
#pragma unroll
    for (int k = 0; k < 6; ++k) acc[k] = sw32sum(acc[k], acc[k + 6]);
    float* dst = &S.red[slotw * 2 + (r4 & 1)][0];
    if (half == 0) {
      *(f32x4*)&dst[8 * c16] = f32x4{acc[0], acc[1], acc[2], acc[3]};
      *(f32x2*)&dst[8 * c16 + 4] = f32x2{acc[4], acc[5]};
    } else {
      *(f32x2*)&dst[8 * c16 + 6] = f32x2{acc[0], acc[1]};
      *(f32x4*)&dst[128 + 4 * c16] = f32x4{acc[2], acc[3], acc[4], acc[5]};
    }
  };

  if (wave >= 8) {
    const int st = tid - 512;
    if (isQ) {
      u32 xr[2][5];
#pragma unroll
      for (int bb = 0; bb < 2; ++bb) {
        const u32* xp = (const u32*)(P.x + bb * D) + lane * 5;
#pragma unroll
        for (int i = 0; i < 5; ++i) xr[bb][i] = xp[i];
      }
      const int s2 = st < 192 ? st : 191, cc = s2 % 24, slot = s2 / 24;
      u32x4 wqr[16];
      {
        const __amdgpu_buffer_rsrc_t rs = mkrsrc(P.wq + (size_t)(jr * 128) * QN + h * DH, 128 * QN * 2);
        const u32 vo = (u32)(slot * 16) * (QN * 2) + cc * 16;
#pragma unroll
        for (int i = 0; i < 16; ++i) wqr[i] = __builtin_amdgcn_raw_buffer_load_b128(rs, vo, i * QN * 2, 2);
      }
      {
        float xv[2][10], sm[2], sq[2];
#pragma unroll
        for (int bb = 0; bb < 2; ++bb) {
#pragma unroll
          for (int i = 0; i < 5; ++i) { xv[bb][2 * i] = lo2f(xr[bb][i]); xv[bb][2 * i + 1] = hi2f(xr[bb][i]); }
          sm[bb] = ((xv[bb][0] + xv[bb][1]) + (xv[bb][2] + xv[bb][3])) + ((xv[bb][4] + xv[bb][5]) + (xv[bb][6] + xv[bb][7])) + (xv[bb][8] + xv[bb][9]);
        }
        sm[0] = wave_sum(sm[0]) * (1.f / D); sm[1] = wave_sum(sm[1]) * (1.f / D);
#pragma unroll
        for (int bb = 0; bb < 2; ++bb) {
          float q0 = 0.f, q1 = 0.f;
#pragma unroll
          for (int i = 0; i < 5; ++i) { const float d0 = xv[bb][2 * i] - sm[bb], d1 = xv[bb][2 * i + 1] - sm[bb]; q0 = fmaf(d0, d0, q0); q1 = fmaf(d1, d1, q1); }
          sq[bb] = q0 + q1;
        }
        sq[0] = wave_sum(sq[0]) * (1.f / D); sq[1] = wave_sum(sq[1]) * (1.f / D);
#pragma unroll
        for (int bb = 0; bb < 2; ++bb) {
          const float mb = rbf(sm[bb]), rden = __builtin_amdgcn_rcpf(rbf(__builtin_amdgcn_sqrtf(rbf(rbf(sq[bb]) + 1e-5f))));
#pragma unroll
          for (int i = 0; i < 5; ++i) S.xnp[bb][lane * 5 + i] = pk2(rbf(xv[bb][2 * i] - mb) * rden, rbf(xv[bb][2 * i + 1] - mb) * rden);
        }
      }
      asm volatile("s_waitcnt lgkmcnt(0)" ::: "memory");
      float a[2][8];
#pragma unroll
      for (int k = 0; k < 8; ++k) { a[0][k] = 0.f; a[1][k] = 0.f; }
#pragma unroll
      for (int i4 = 0; i4 < 2; ++i4) {
        const u32x4 x0 = *(const u32x4*)&S.xnp[0][jr * 64 + slot * 8 + i4 * 4];
        const u32x4 x1 = *(const u32x4*)&S.xnp[1][jr * 64 + slot * 8 + i4 * 4];
#pragma unroll
        for (int i = 0; i < 4; ++i) {
          const u32x4 w0 = wqr[(i4 * 4 + i) * 2], w1 = wqr[(i4 * 4 + i) * 2 + 1];
#pragma unroll
          for (int k = 0; k < 4; ++k) {
            const u32 wl = plo(w0[k], w1[k]), wh = phi(w0[k], w1[k]);
            a[0][2 * k] = dot2(wl, x0[i], a[0][2 * k]); a[0][2 * k + 1] = dot2(wh, x0[i], a[0][2 * k + 1]);
            a[1][2 * k] = dot2(wl, x1[i], a[1][2 * k]); a[1][2 * k + 1] = dot2(wh, x1[i], a[1][2 * k + 1]);
          }
        }
      }
      if (st < 192) {
#pragma unroll
        for (int bb = 0; bb < 2; ++bb) {
          *(f32x4*)&S.qpart[slot][bb][cc * 8] = f32x4{a[bb][0], a[bb][1], a[bb][2], a[bb][3]};
          *(f32x4*)&S.qpart[slot][bb][cc * 8 + 4] = f32x4{a[bb][4], a[bb][5], a[bb][6], a[bb][7]};
        }
      }
      asm volatile("s_waitcnt lgkmcnt(0)" ::: "memory");
      if (lane == 0) __hip_atomic_fetch_add(&S.qcnt, 1u, __ATOMIC_RELAXED, __HIP_MEMORY_SCOPE_WORKGROUP);
      while (LDS_LD(&S.qcnt) != 4u) __builtin_amdgcn_s_sleep(1);
      asm volatile("" ::: "memory");
      GET_TAGS();
      if (st < 192) {
        const int bb = st / 96, k = st % 96;
        float s0 = 0.f, s1 = 0.f;
#pragma unroll
        for (int w = 0; w < 8; ++w) { const f32x2 t = *(const f32x2*)&S.qpart[w][bb][2 * k]; s0 += t[0]; s1 += t[1]; }
        st_gran(P.ws + WS_Q + ((size_t)((h * 2 + bb) * 5 + jr) * 96 + k) * 16, s0, s1, ta, tb);
      }
    }
    if (wave == 8) {
      const unsigned char* pq = P.ws + WS_Q + (size_t)(h * 2 + b) * 480 * 16;
      const unsigned char* p0 = pq + lane * 16;
      const unsigned char* p1 = pq + (64 + c) * 16;
      u32x4 g[10];
      GET_TAGS();
      for (int it = 0; it < SPIN_LIMIT; ++it) {
        ld_gran10(p0, p0 + 4608, p1, p1 + 4608, g);
        bool ok = true;
#pragma unroll
        for (int i = 0; i < 10; ++i) ok = ok && TAGOK(g[i]);
        if (__all(ok)) break;
        __builtin_amdgcn_s_sleep(2);
      }
      float s0 = 0.f, s1 = 0.f, t0 = 0.f, t1 = 0.f;
#pragma unroll
      for (int i = 0; i < 5; ++i) {
        s0 += __uint_as_float(g[i][0]); s1 += __uint_as_float(g[i][2]);
        t0 += __uint_as_float(g[5 + i][0]); t1 += __uint_as_float(g[5 + i][2]);
      }
      S.qpk[lane] = pk2(s0, s1);
      if (lane < 32) S.qpk[64 + lane] = pk2(t0, t1);
      asm volatile("s_waitcnt lgkmcnt(0)" ::: "memory");
      if (lane == 0) __hip_atomic_store(&S.qflag, 1u, __ATOMIC_RELAXED, __HIP_MEMORY_SCOPE_WORKGROUP);
    } else {
      const int tw = st - 64;
      if (isO) {
        const __amdgpu_buffer_rsrc_t ro = mkrsrc(P.wo + (size_t)(h * DH + jr * 32) * D, 32 * D * 2);
        u32x4 w0[7], w1[7];
#pragma unroll
        for (int i = 0; i < 7; ++i) {
          const int task = tw + 192 * i, k = task / 80, ch = task % 80;
          w0[i] = __builtin_amdgcn_raw_buffer_load_b128(ro, (u32)(2 * k) * (D * 2) + ch * 16, 0, 2);
          w1[i] = __builtin_amdgcn_raw_buffer_load_b128(ro, (u32)(2 * k) * (D * 2) + ch * 16, D * 2, 2);
        }
#pragma unroll
        for (int i = 0; i < 7; ++i) {
          const int task = tw + 192 * i, k = task / 80, ch = task % 80;
          if (task < 1280) {
            u32* d = &S.wst[k * 640 + ch * 8];
            *(u32x4*)d = u32x4{plo(w0[i][0], w1[i][0]), phi(w0[i][0], w1[i][0]), plo(w0[i][1], w1[i][1]), phi(w0[i][1], w1[i][1])};
            *(u32x4*)(d + 4) = u32x4{plo(w0[i][2], w1[i][2]), phi(w0[i][2], w1[i][2]), plo(w0[i][3], w1[i][3]), phi(w0[i][3], w1[i][3])};
          }
        }
      } else {
        const __amdgpu_buffer_rsrc_t rd = mkrsrc(P.wd + (size_t)(fidx * 24) * D, 24 * D * 2);
        u32x4 w0[5], w1[5];
#pragma unroll
        for (int i = 0; i < 5; ++i) {
          const int task = tw + 192 * i, k = task / 80, ch = task % 80;
          w0[i] = __builtin_amdgcn_raw_buffer_load_b128(rd, (u32)(2 * k) * (D * 2) + ch * 16, 0, 2);
          w1[i] = __builtin_amdgcn_raw_buffer_load_b128(rd, (u32)(2 * k) * (D * 2) + ch * 16, D * 2, 2);
        }
#pragma unroll
        for (int i = 0; i < 5; ++i) {
          const int task = tw + 192 * i, k = task / 80, ch = task % 80;
          u32* d = &S.wst[48 * GUS + k * 640 + ch * 8];
          *(u32x4*)d = u32x4{plo(w0[i][0], w1[i][0]), phi(w0[i][0], w1[i][0]), plo(w0[i][1], w1[i][1]), phi(w0[i][1], w1[i][1])};
          *(u32x4*)(d + 4) = u32x4{plo(w0[i][2], w1[i][2]), phi(w0[i][2], w1[i][2]), plo(w0[i][3], w1[i][3]), phi(w0[i][3], w1[i][3])};
        }
      }
    }
  } else {
    stream_body(wave, 16, wave * 64, true);
  }
  __syncthreads();
  GET_TAGS();
  float hres0 = 0.f, hres1 = 0.f;
  const int t3 = tid < 320 ? tid : 319;
  const int gslot = (tid >> 2) * 8 + (tid & 3);
  u32x2 wpre[16];
  if (wave < 2) {
    float mm[8];
    float Mwg = -INFINITY;
#pragma unroll
    for (int w = 0; w < 8; ++w) { mm[w] = S.ml[w][0]; Mwg = fmaxf(Mwg, mm[w]); }
    float Lwg = 0.f;
#pragma unroll
    for (int w = 0; w < 8; ++w) { mm[w] = __builtin_amdgcn_exp2f(mm[w] - Mwg); Lwg = fmaf(mm[w], S.ml[w][1], Lwg); }
    const int t = tid < 97 ? (tid > 0 ? tid - 1 : 0) : 95;
    float o0 = 0.f, o1 = 0.f;
#pragma unroll
    for (int w = 0; w < 8; ++w) {
      const f32x2 a = *(const f32x2*)&S.red[2 * w][2 * t], bb = *(const f32x2*)&S.red[2 * w + 1][2 * t];
      o0 = fmaf(mm[w], a[0] + bb[0], o0); o1 = fmaf(mm[w], a[1] + bb[1], o1);
    }
    if (tid == 0) { o0 = Mwg; o1 = Lwg; }
    if (tid < 97) {
      st_gran(P.ws + WS_O + ((size_t)prod * 128 + tid) * 16, o0, o1, ta, tb);
      *(f32x2*)&S.oown[tid * 2] = f32x2{o0, o1};
    }
    asm volatile("s_waitcnt lgkmcnt(0)" ::: "memory");
    if (lane == 0) __hip_atomic_fetch_add(&S.ocnt, 1u, __ATOMIC_RELAXED, __HIP_MEMORY_SCOPE_WORKGROUP);
  } else if (isO && wave < 4) {
    const int bb = wave - 2, pj = bb * 4 + r4;
    const bool own = pj == j8;
    const unsigned char* pb = P.ws + WS_O + (size_t)(h * 8 + pj) * 128 * 16;
    u32x4 go, gm;
    for (int it = 0; it < SPIN_LIMIT; ++it) {
      ld_gran2(pb + (1 + jr * 16 + c16) * 16, pb, go, gm);
      if (__all(own || (TAGOK(go) && TAGOK(gm)))) break;
    }
    while (LDS_LD(&S.ocnt) != 2u) __builtin_amdgcn_s_sleep(1);
    asm volatile("" ::: "memory");
    const f32x2 om = *(const f32x2*)&S.oown[0], ov = *(const f32x2*)&S.oown[(1 + jr * 16 + c16) * 2];
    const float m = own ? om[0] : __uint_as_float(gm[0]), l = own ? om[1] : __uint_as_float(gm[2]);
    const float v0 = own ? ov[0] : __uint_as_float(go[0]), v1 = own ? ov[1] : __uint_as_float(go[2]);
    const float M = x1632max(m);
    const float e = __builtin_amdgcn_exp2f(m - M);
    const float L = x1632sum(e * l);
    const float a0 = x1632sum(e * v0), a1 = x1632sum(e * v1);
    const float rl = __builtin_amdgcn_rcpf(L);
    if (lane < 16) S.opk[bb][lane] = pk2(a0 * rl, a1 * rl);
    asm volatile("s_waitcnt lgkmcnt(0)" ::: "memory");
    if (lane == 0) __hip_atomic_fetch_add(&S.oflag, 1u, __ATOMIC_RELAXED, __HIP_MEMORY_SCOPE_WORKGROUP);
  } else if (isO && wave >= 7) {
    const int yt = tid - 448;
#pragma unroll
    for (int k = 0; k < 16; ++k) wpre[k] = *(const u32x2*)&S.wst[k * 640 + 2 * yt];
    while (LDS_LD(&S.oflag) != 2u) __builtin_amdgcn_s_sleep(1);
    asm volatile("" ::: "memory");
    float y00 = 0.f, y01 = 0.f, y10 = 0.f, y11 = 0.f;
#pragma unroll
    for (int k4 = 0; k4 < 4; ++k4) {
      const u32x4 oa = *(const u32x4*)&S.opk[0][k4 * 4], ob = *(const u32x4*)&S.opk[1][k4 * 4];
#pragma unroll
      for (int k = 0; k < 4; ++k) {
        const u32x2 w = wpre[k4 * 4 + k];
        y00 = dot2(w[0], oa[k], y00); y01 = dot2(w[1], oa[k], y01);
        y10 = dot2(w[0], ob[k], y10); y11 = dot2(w[1], ob[k], y11);
      }
    }
    unsigned char* py = P.ws + WS_Y + ((size_t)(h * 6 + jr) * 640 + (yt >> 2) * 8 + (yt & 3)) * 16;
    st_gran(py, y00, y01, ta, tb);
    st_gran(py + 64, y10, y11, ta, tb);
    if (lane == 0) __hip_atomic_fetch_add(&S.ycnt, 1u, __ATOMIC_RELAXED, __HIP_MEMORY_SCOPE_WORKGROUP);
  }
  if (isR && wave < 6) {
    const unsigned char* p0 = P.ws + WS_Y + ((size_t)(tid >> 3) * 640 + rid * 8 + (tid & 7)) * 16;
    constexpr size_t ST = (size_t)48 * 640 * 16;
    u32x4 ga, gb, gc, gd;
    while (LDS_LD(&S.ycnt) != 5u) __builtin_amdgcn_s_sleep(2);
    for (int it = 0; it < SPIN_LIMIT; ++it) {
      ld_gran4(p0, p0 + ST, p0 + 2 * ST, p0 + 3 * ST, ga, gb, gc, gd);
      if (__all(TAGOK(ga) && TAGOK(gb) && TAGOK(gc) && TAGOK(gd))) break;
      __builtin_amdgcn_s_sleep(1);
    }
    float y0 = (__uint_as_float(ga[0]) + __uint_as_float(gb[0])) + (__uint_as_float(gc[0]) + __uint_as_float(gd[0]));
    float y1 = (__uint_as_float(ga[2]) + __uint_as_float(gb[2])) + (__uint_as_float(gc[2]) + __uint_as_float(gd[2]));
    y0 += dpp<XOR8>(y0); y1 += dpp<XOR8>(y1);
    y0 = x1632sum(y0); y1 = x1632sum(y1);
    if (lane < 8) *(f32x2*)&S.wsum[wave][lane * 2] = f32x2{y0, y1};
    asm volatile("s_waitcnt lgkmcnt(0)" ::: "memory");
    if (lane == 0) __hip_atomic_fetch_add(&S.hcnt, 1u, __ATOMIC_RELAXED, __HIP_MEMORY_SCOPE_WORKGROUP);
    if (wave == 0) {
      while (LDS_LD(&S.hcnt) != 6u) __builtin_amdgcn_s_sleep(1);
      asm volatile("" ::: "memory");
      if (tid < 8) {
        float a0 = 0.f, a1 = 0.f;
#pragma unroll
        for (int w = 0; w < 6; ++w) { a0 += S.wsum[w][tid * 2]; a1 += S.wsum[w][tid * 2 + 1]; }
        hres0 = rbf(lo2f(xres) + rbf(a0)); hres1 = rbf(hi2f(xres) + rbf(a1));
        st_gran(P.ws + WS_H + (size_t)(rid * 8 + tid) * 16, hres0, hres1, ta, tb);
      }
      if (lane == 0) __hip_atomic_store(&S.hflag, 1u, __ATOMIC_RELAXED, __HIP_MEMORY_SCOPE_WORKGROUP);
    }
  }
  if (isF) {
    if (wave >= 8) {
      while (LDS_LD(&S.ocnt) != 2u) __builtin_amdgcn_s_sleep(1);
      asm volatile("" ::: "memory");
      const int st = tid - 512;
#pragma unroll
      for (int m = 0; m < 2; ++m) {
        {
          const __amdgpu_buffer_rsrc_t ru = mkrsrc((m ? P.wu : P.wg) + fidx * 24, (D * F - fidx * 24) * 2);
          u32x4 a0[4], a1[4];
#pragma unroll
          for (int i = 0; i < 4; ++i) {
            const int task = st + 256 * i, k = task / 3, q = task % 3;
            const u32 vo = task < 960 ? (u32)(2 * k) * (F * 2) + q * 16 : 0x7fffff00u;
            a0[i] = __builtin_amdgcn_raw_buffer_load_b128(ru, vo, 0, 0);
            a1[i] = __builtin_amdgcn_raw_buffer_load_b128(ru, vo, F * 2, 0);
          }
#pragma unroll
          for (int i = 0; i < 4; ++i) {
            const int task = st + 256 * i, k = task / 3, q = task % 3;
            if (task < 960) {
#pragma unroll
              for (int e = 0; e < 4; ++e) {
                S.wst[(m * 24 + q * 8 + 2 * e) * GUS + k] = plo(a0[i][e], a1[i][e]);
                S.wst[(m * 24 + q * 8 + 2 * e + 1) * GUS + k] = phi(a0[i][e], a1[i][e]);
              }
            }
          }
        }
      }
    }
    __syncthreads();
    u32x4 wreg[5];
    {
      const u32* wrow = &S.wst[((r4 >> 1) * 24 + 2 * wave + (r4 & 1)) * GUS + 20 * c16];
#pragma unroll
      for (int i = 0; i < 5; ++i) wreg[i] = *(const u32x4*)&wrow[4 * i];
#pragma unroll
      for (int k = 0; k < 12; ++k) wpre[k] = *(const u32x2*)&S.wst[48 * GUS + k * 640 + 2 * t3];
    }
    float cs = 0.f;
#pragma unroll
    for (int i = 0; i < 5; ++i)
#pragma unroll
      for (int k = 0; k < 4; ++k) cs = dot2(wreg[i][k], 0x3f803f80u, cs);
    cs = row16sum(cs);
    if (tid < 320) {
      const unsigned char* p0 = P.ws + WS_H + (size_t)gslot * 16;
      u32x4 ga, gb;
      for (int it = 0; it < SPIN_LIMIT; ++it) {
        ld_gran2(p0, p0 + 64, ga, gb);
        if (__all(TAGOK(ga) && TAGOK(gb))) break;
      }
      const float h00 = __uint_as_float(ga[0]), h01 = __uint_as_float(ga[2]), h10 = __uint_as_float(gb[0]), h11 = __uint_as_float(gb[2]);
      S.hp[0][tid] = (ga[0] >> 16) | (ga[2] & 0xffff0000u);
      S.hp[1][tid] = (gb[0] >> 16) | (gb[2] & 0xffff0000u);
      float s0 = h00 + h01, q0 = h00 * h00 + h01 * h01, s1 = h10 + h11, q1 = h10 * h10 + h11 * h11;
      s0 = sw32sum(s0, s1); q0 = sw32sum(q0, q1);
      s0 = sw16sum(s0, q0);
      s0 = row16sum(s0);
      if (c16 == 0) S.st[wave][r4] = s0;
    }
    __syncthreads();
    {
      const int bsel = c16 & 1;
      float d0 = 0.f, d1 = 0.f;
#pragma unroll
      for (int i = 0; i < 5; ++i) {
        const u32x4 ha = *(const u32x4*)&S.hp[0][20 * c16 + 4 * i], hb = *(const u32x4*)&S.hp[1][20 * c16 + 4 * i];
#pragma unroll
        for (int k = 0; k < 4; ++k) { d0 = dot2(wreg[i][k], ha[k], d0); d1 = dot2(wreg[i][k], hb[k], d1); }
      }
      float sm = 0.f, sq = 0.f;
#pragma unroll
      for (int w = 0; w < 5; ++w) { const f32x2 t = *(const f32x2*)&S.st[w][bsel * 2]; sm += t[0]; sq += t[1]; }
      const float mean = sm * (1.f / D);
      const float var = fmaxf(sq * (1.f / D) - mean * mean, 0.f);
      const float mb = rbf(mean), den = rbf(__builtin_amdgcn_sqrtf(rbf(rbf(var) + 1e-5f)));
      const float rden = __builtin_amdgcn_rcpf(den);
      float dd = (bsel ? d1 : d0) + dpp<XOR1>(bsel ? d0 : d1);
      dd += dpp<XOR2>(dd); dd += dpp<XOR1>(dpp<XOR7>(dd)); dd += dpp<XOR8>(dd);
      const float mine = rbf((dd - mb * cs) * rden);
      const float up = upper32(mine);
      const float t = 0.7978845608028654f * (mine + 0.044715f * mine * mine * mine);
      const float th = 1.f - 2.f * __builtin_amdgcn_rcpf(__builtin_amdgcn_exp2f(t * 2.8853900817779268f) + 1.f);
      const float gval = rbf(rbf(0.5f * mine * (1.f + th)) * up);
      if (half == 0 && c16 < 2) ((uint16_t*)&S.gp[c16][0])[2 * wave + r4] = (uint16_t)(__float_as_uint(gval) >> 16);
    }
    __syncthreads();
    if (tid < 320) {
      float z00 = 0.f, z01 = 0.f, z10 = 0.f, z11 = 0.f;
#pragma unroll
      for (int k4 = 0; k4 < 3; ++k4) {
        const u32x4 oa = *(const u32x4*)&S.gp[0][k4 * 4], ob = *(const u32x4*)&S.gp[1][k4 * 4];
#pragma unroll
        for (int k = 0; k < 4; ++k) {
          const u32x2 w = wpre[k4 * 4 + k];
          z00 = dot2(w[0], oa[k], z00); z01 = dot2(w[1], oa[k], z01);
          z10 = dot2(w[0], ob[k], z10); z11 = dot2(w[1], ob[k], z11);
        }
      }
      unsigned char* pz = P.ws + WS_Z + ((size_t)fidx * 640 + gslot) * 16;
      st_gran(pz, z00, z01, ta, tb);
      st_gran(pz + 64, z10, z11, ta, tb);
    }
  }
  if (isR && wave < 9) {
    if (wave > 0) {
      const int zt = tid - 64;
      const unsigned char* p0 = P.ws + WS_Z + ((size_t)(zt >> 3) * 640 + rid * 8 + (zt & 7)) * 16;
      u32x4 ga;
      while (LDS_LD(&S.hflag) == 0u) __builtin_amdgcn_s_sleep(2);
      for (int it = 0; it < SPIN_LIMIT; ++it) {
        ld_gran1(p0, ga);
        if (__all(TAGOK(ga))) break;
        __builtin_amdgcn_s_sleep(1);
      }
      float y0 = __uint_as_float(ga[0]), y1 = __uint_as_float(ga[2]);
      y0 += dpp<XOR8>(y0); y1 += dpp<XOR8>(y1);
      y0 = x1632sum(y0); y1 = x1632sum(y1);
      if (lane < 8) *(f32x2*)&S.zsum[wave - 1][lane * 2] = f32x2{y0, y1};
      asm volatile("s_waitcnt lgkmcnt(0)" ::: "memory");
      if (lane == 0) __hip_atomic_fetch_add(&S.zcnt, 1u, __ATOMIC_RELAXED, __HIP_MEMORY_SCOPE_WORKGROUP);
    } else {
      while (LDS_LD(&S.zcnt) != 8u) __builtin_amdgcn_s_sleep(1);
      asm volatile("" ::: "memory");
      if (tid < 8) {
        float a0 = 0.f, a1 = 0.f;
#pragma unroll
        for (int w = 0; w < 8; ++w) { a0 += S.zsum[w][tid * 2]; a1 += S.zsum[w][tid * 2 + 1]; }
        const u32 o = pk2(hres0 + rbf(a0), hres1 + rbf(a1));
        ((u32*)(P.out + (tid >> 2) * D + rid * 8))[tid & 3] = o;
      }
      if (wg == 0 && tid == 0) {
        u32x2 ne = {ta, tb};
        asm volatile("global_store_dwordx2 %0, %1, off sc1" :: "v"(P.ws + WS_EPOCH), "v"(ne) : "memory");
      }
    }
  }
}
}

extern "C" void kernel_launch(void* const* d_in, const int* in_sizes, int n_in, void* d_out, int out_size,
                              void* d_ws, size_t ws_size, hipStream_t stream) {
  Params P;
  P.x = (const uint16_t*)d_in[0]; P.kc = (const uint16_t*)d_in[1]; P.vc = (const uint16_t*)d_in[2];
  P.wq = (const uint16_t*)d_in[3]; P.wo = (const uint16_t*)d_in[4];
  P.wg = (const uint16_t*)d_in[6]; P.wu = (const uint16_t*)d_in[7]; P.wd = (const uint16_t*)d_in[8];
  P.out = (uint16_t*)d_out; P.ws = (unsigned char*)d_ws;
  fused_kernel<<<NWG, NT, 0, stream>>>(P);
}
```
